```python
import torch

def _softmax(x, axis=-1):
    return torch.softmax(x, dim=axis)
def _gelu(x):
    return torch.nn.functional.gelu(x, approximate="tanh")
def _silu(x):
    return torch.nn.functional.silu(x)
def _sigmoid(x):
    return torch.sigmoid(x)
def _layernorm(x, axis=-1, eps=1e-5):
    m = x.mean(dim=axis, keepdim=True)
    v = x.var(dim=axis, keepdim=True, unbiased=False)
    return (x - m) / torch.sqrt(v + eps)
def _scatter_add(tgt, idx, src, axis=0):
    return tgt.clone().index_add_(axis, idx, src)
def _segment_sum(src, seg, n):
    out = torch.zeros((n,) + src.shape[1:], dtype=src.dtype)
    return out.index_add_(0, seg, src)
def _pad(x, pw):
    flat = tuple(v for pair in reversed(pw) for v in pair)
    return torch.nn.functional.pad(x, flat)

kernel_name = 'sequential_preLN__sliding_local_dilated__dense_activation_relu__prompt'

def setup_inputs():

    _w = (lambda a: (lambda t: t.to(torch.bfloat16) if t.is_floating_point() else t)(torch.as_tensor(a)))
    def _A_setup():
        B, S, D = 1, 2048, 512
        return {
            "x": _w((lambda *s: torch.randn(*s, dtype=torch.bfloat16) * 0.05)(B, S, D)),
            "wqkv": _w((lambda *s: torch.randn(*s, dtype=torch.bfloat16) * 0.05)(D, 3 * D)),
            "wo": _w((lambda *s: torch.randn(*s, dtype=torch.bfloat16) * 0.05)(D, D)),
        }
    _A_in = _A_setup()

    def _B_setup():
        B, S, D, Dff = 1, 2048, 512, 1376
        return {
            "x": _w((lambda *s: torch.randn(*s, dtype=torch.bfloat16) * 0.05)(B, S, D)),
            "w1": _w((lambda *s: torch.randn(*s, dtype=torch.bfloat16) * 0.05)(D, Dff)),
            "w2": _w((lambda *s: torch.randn(*s, dtype=torch.bfloat16) * 0.05)(Dff, D)),
        }
    _B_in = _B_setup()

    return {
        "A_x": _A_in["x"],
        "A_wqkv": _A_in["wqkv"],
        "A_wo": _A_in["wo"],
        "B_x": _B_in["x"],
        "B_w1": _B_in["w1"],
        "B_w2": _B_in["w2"],
    }

def reference(A_x, A_wqkv, A_wo, B_x, B_w1, B_w2):
    def _A_ref(x, wqkv, wo):
        B, S, D = x.shape
        H = 32; Dh = D // H
        W, dil = 8, 2
        qkv = (lambda x, *d: x.permute(*d))((x @ wqkv).reshape(B, S, 3, H, Dh), 2, 0, 3, 1, 4)
        q, k, v = qkv[0], qkv[1], qkv[2]
        scale = Dh ** -0.5
        pos = torch.arange(S)
        off = torch.arange(W) * dil
        idx = pos[:, None] - off[None, :]
        mask = idx >= 0
        idx_c = torch.where(mask, idx, 0).reshape(-1)
        kg = k[:, :, idx_c, :].reshape(B, H, S, W, Dh)
        vg = v[:, :, idx_c, :].reshape(B, H, S, W, Dh)
        logits = torch.einsum("bhsd,bhswd->bhsw", q, kg) * scale
        logits = torch.where(mask[None, None, :, :], logits, -1e9)
        attn = _softmax(logits, axis=-1)
        o = torch.einsum("bhsw,bhswd->bhsd", attn, vg)
        return (lambda x, *d: x.permute(*d))(o, 0, 2, 1, 3).reshape(B, S, D) @ wo

    def _B_ref(x, w1, w2):
        h = torch.einsum("bsd,df->bsf", x, w1)
        zero = torch.zeros_like(h)
        h = torch.maximum(h, zero)
        return torch.einsum("bsf,fd->bsd", h, w2)

    x = A_x
    h = x + _A_ref(_layernorm(x, axis=-1), A_wqkv, A_wo)
    h2 = h + _B_ref(_layernorm(h, axis=-1), B_w1, B_w2)
    return h2
```

```cpp
#include <hip/hip_runtime.h>
#include <hip/hip_bf16.h>
#include <stdint.h>
typedef unsigned short u16;
using bf16x8 = __attribute__((ext_vector_type(8))) short;
using f32x4 = __attribute__((ext_vector_type(4))) float;

__device__ __forceinline__ float bf2f(u16 v) { return __uint_as_float(((unsigned)v) << 16); }
__device__ __forceinline__ u16 f2bf(float f) { __hip_bfloat16 b = __float2bfloat16(f); return *(u16*)&b; }
__device__ __forceinline__ float rbf(float f) { return bf2f(f2bf(f)); }

constexpr int S = 2048, D = 512, DFF = 1376;

__global__ void k_ln(const u16* __restrict__ x, u16* __restrict__ y) {
    int row = blockIdx.x * 4 + (threadIdx.x >> 6), lane = threadIdx.x & 63;
    const u16* p = x + (size_t)row * D + lane * 8;
    float v[8]; float s = 0;
    for (int i = 0; i < 8; i++) { v[i] = bf2f(p[i]); s += v[i]; }
    for (int o = 32; o; o >>= 1) s += __shfl_xor(s, o);
    float m = s / D; float q = 0;
    for (int i = 0; i < 8; i++) { float d = v[i] - m; q += d * d; }
    for (int o = 32; o; o >>= 1) q += __shfl_xor(q, o);
    float var = q / D;
    float r = 1.0f / sqrtf(var + 1e-5f);
    for (int i = 0; i < 8; i++) y[(size_t)row * D + lane * 8 + i] = f2bf((v[i] - m) * r);
}

template <int MODE>
__global__ void k_gemm(const u16* __restrict__ A, const u16* __restrict__ B, u16* __restrict__ C, const u16* __restrict__ res, int M, int N, int K) {
    int lane = threadIdx.x & 63, w = threadIdx.x >> 6;
    int m0 = blockIdx.y * 64 + w * 16, n0 = blockIdx.x * 32;
    f32x4 acc[2] = {};
    for (int k0 = 0; k0 < K; k0 += 32) {
        bf16x8 a = *(const bf16x8*)(A + (size_t)(m0 + (lane & 15)) * K + k0 + 8 * (lane >> 4));
        for (int t = 0; t < 2; t++) {
            bf16x8 b;
            for (int j = 0; j < 8; j++) b[j] = B[(size_t)(k0 + 8 * (lane >> 4) + j) * N + n0 + t * 16 + (lane & 15)];
            acc[t] = __builtin_amdgcn_mfma_f32_16x16x32_bf16(a, b, acc[t], 0, 0, 0);
        }
    }
    for (int t = 0; t < 2; t++) for (int r = 0; r < 4; r++) {
        int row = m0 + (lane >> 4) * 4 + r, col = n0 + t * 16 + (lane & 15);
        float v = acc[t][r];
        if (MODE == 1) v = fmaxf(v, 0.f);
        if (MODE == 2) v = rbf(v) + bf2f(res[(size_t)row * N + col]);
        C[(size_t)row * N + col] = f2bf(v);
    }
}

__global__ void k_attn(const u16* __restrict__ qkv, u16* __restrict__ o) {
    int t = blockIdx.x * blockDim.x + threadIdx.x;
    int s = t >> 5, h = t & 31;
    float q[16];
    for (int d = 0; d < 16; d++) q[d] = bf2f(qkv[(size_t)s * 1536 + h * 16 + d]);
    float lg[8]; float mx = -3e38f;
    for (int w = 0; w < 8; w++) {
        int p = s - 2 * w;
        float a = -1e9f;
        if (p >= 0) {
            a = 0;
            for (int d = 0; d < 16; d++) a += q[d] * bf2f(qkv[(size_t)p * 1536 + 512 + h * 16 + d]);
            a = rbf(a) * 0.25f;
        }
        lg[w] = a; mx = fmaxf(mx, a);
    }
    float sum = 0;
    for (int w = 0; w < 8; w++) { lg[w] = __expf(lg[w] - mx); sum += lg[w]; }
    float acc[16] = {};
    for (int w = 0; w < 8; w++) {
        int p = s - 2 * w; if (p < 0) p = 0;
        float pw = rbf(lg[w] / sum);
        for (int d = 0; d < 16; d++) acc[d] += pw * bf2f(qkv[(size_t)p * 1536 + 1024 + h * 16 + d]);
    }
    for (int d = 0; d < 16; d++) o[(size_t)s * 512 + h * 16 + d] = f2bf(acc[d]);
}

extern "C" void kernel_launch(void* const* d_in, const int* in_sizes, int n_in, void* d_out, int out_size, void* d_ws, size_t ws_size, hipStream_t stream) {
    const u16* x = (const u16*)d_in[0]; const u16* wqkv = (const u16*)d_in[1]; const u16* wo = (const u16*)d_in[2];
    const u16* w1 = (const u16*)d_in[4]; const u16* w2 = (const u16*)d_in[5];
    u16* ws = (u16*)d_ws;
    u16* ln = ws; u16* qkv = ln + S * D; u16* o = qkv + S * 1536; u16* h = o + S * D; u16* f = h + S * D;
    u16* out = (u16*)d_out;
    k_ln<<<S / 4, 256, 0, stream>>>(x, ln);
    k_gemm<0><<<dim3(1536 / 32, S / 64), 256, 0, stream>>>(ln, wqkv, qkv, nullptr, S, 1536, 512);
    k_attn<<<S * 32 / 256, 256, 0, stream>>>(qkv, o);
    k_gemm<2><<<dim3(512 / 32, S / 64), 256, 0, stream>>>(o, wo, h, x, S, 512, 512);
    k_ln<<<S / 4, 256, 0, stream>>>(h, ln);
    k_gemm<1><<<dim3(DFF / 32, S / 64), 256, 0, stream>>>(ln, w1, f, nullptr, S, DFF, 512);
    k_gemm<2><<<dim3(512 / 32, S / 64), 256, 0, stream>>>(f, w2, out, h, S, 512, DFF);
}
```

```cpp
#include <hip/hip_runtime.h>
#include <hip/hip_bf16.h>
#include <stdint.h>

typedef unsigned short u16;
typedef unsigned long long u64;
typedef __attribute__((ext_vector_type(8))) short s16x8;
typedef __attribute__((ext_vector_type(4))) short s16x4;
typedef __attribute__((ext_vector_type(4))) float f32x4;
typedef __attribute__((ext_vector_type(2))) float f32x2;
typedef __attribute__((ext_vector_type(4))) unsigned u32x4;
typedef __attribute__((ext_vector_type(2))) unsigned u32x2;
typedef __attribute__((ext_vector_type(2))) __bf16 bf16x2;
#define GAS __attribute__((address_space(1)))
#define LAS __attribute__((address_space(3)))
typedef GAS unsigned gu32;
typedef GAS u64 gu64;
#define RLX_AGENT __ATOMIC_RELAXED, __HIP_MEMORY_SCOPE_AGENT
#define DEV __device__ __forceinline__

namespace {
constexpr int S_ = 2048, D_ = 512, DFF = 1376;
constexpr unsigned WS_EPOCH = 0;
constexpr unsigned WS_GFLAG = 4096;
constexpr unsigned WS_SFLAG = 8192;
constexpr unsigned WS_DIAG  = 65536;
constexpr unsigned WS_OT  = 1u << 20;
constexpr unsigned WS_HT  = 3u << 20;
constexpr unsigned WS_FT  = 5u << 20;
constexpr unsigned WS_WOT = 11u << 20;
constexpr unsigned WS_W1T = 12u << 20;
constexpr unsigned WS_W2T = 14u << 20;
constexpr unsigned WS_USED = 16u << 20;
constexpr unsigned L_A = 0;
constexpr unsigned L_B = 81920;
constexpr unsigned L_PT = 131072;
constexpr unsigned L_ST = 147456;
constexpr unsigned L_CS = 148480;
constexpr unsigned L_XP = 149504;
constexpr unsigned L_X3 = 98304;
constexpr unsigned L_S3 = 131072;
constexpr unsigned L_DG = 151552;
constexpr unsigned LDS_BYTES = 151552 + 512;
constexpr unsigned SPIN_CAP = 1u << 16;

DEV float bf_lo(unsigned v) { return __uint_as_float(v << 16); }
DEV float bf_hi(unsigned v) { return __uint_as_float(v & 0xffff0000u); }
DEV unsigned pk_bf16(float a, float b) {
    bf16x2 r; r[0] = (__bf16)a; r[1] = (__bf16)b; return __builtin_bit_cast(unsigned, r);
}
DEV float rbf(float a) { return (float)((__bf16)a); }
DEV float dot2(unsigned a, unsigned b, float c) {
    return __builtin_amdgcn_fdot2_f32_bf16(__builtin_bit_cast(bf16x2, a), __builtin_bit_cast(bf16x2, b), c, false);
}
DEV f32x4 mfma32(u32x4 a, u32x4 b, f32x4 c) {
    return __builtin_amdgcn_mfma_f32_16x16x32_bf16(__builtin_bit_cast(s16x8, a), __builtin_bit_cast(s16x8, b), c, 0, 0, 0);
}
DEV f32x4 mfma16(u32x2 a, u32x2 b, f32x4 c) {
    return __builtin_amdgcn_mfma_f32_16x16x16bf16_1k(__builtin_bit_cast(s16x4, a), __builtin_bit_cast(s16x4, b), c, 0, 0, 0);
}
DEV u32x2 tr_read(LAS char* p) { return __builtin_bit_cast(u32x2, __builtin_amdgcn_ds_read_tr16_b64_v4i16((LAS s16x4*)p)); }
DEV u32x4 lds_r128(LAS char* p) { return *(LAS u32x4*)p; }
DEV void lds_w128(LAS char* p, u32x4 v) { *(LAS u32x4*)p = v; }
template <int CTRL> DEV float dpp_add(float v) {
    return v + __uint_as_float(__builtin_amdgcn_update_dpp(0, __float_as_uint(v), CTRL, 0xf, 0xf, true));
}
DEV float xsum16_32(float v) {
    auto a = __builtin_amdgcn_permlane16_swap(__float_as_uint(v), __float_as_uint(v), false, false);
    v = __uint_as_float(a[0]) + __uint_as_float(a[1]);
    auto b = __builtin_amdgcn_permlane32_swap(__float_as_uint(v), __float_as_uint(v), false, false);
    return __uint_as_float(b[0]) + __uint_as_float(b[1]);
}
DEV float xmax16_32(float v) {
    auto a = __builtin_amdgcn_permlane16_swap(__float_as_uint(v), __float_as_uint(v), false, false);
    v = fmaxf(__uint_as_float(a[0]), __uint_as_float(a[1]));
    auto b = __builtin_amdgcn_permlane32_swap(__float_as_uint(v), __float_as_uint(v), false, false);
    return fmaxf(__uint_as_float(b[0]), __uint_as_float(b[1]));
}
DEV f32x2 ln_stats(float s, float q) {
    float m = s * (1.0f / 512.0f);
    float var = fmaxf(q * (1.0f / 512.0f) - m * m, 0.0f);
    float sd = rbf(__builtin_sqrtf(rbf(rbf(var) + 1e-5f)));
    f32x2 r; r[0] = rbf(m); r[1] = 1.0f / sd; return r;
}
DEV void bar() { asm volatile("s_waitcnt lgkmcnt(0)" ::: "memory"); __builtin_amdgcn_s_barrier(); asm volatile("" ::: "memory"); }
DEV void drain() { asm volatile("s_waitcnt vmcnt(0)" ::: "memory"); }
DEV void fence() { asm volatile("" ::: "memory"); }
#define STAMP(i) do {} while (0)
}

__global__ __launch_bounds__(512, 2) void fused_block_kernel(const u16* __restrict__ x, const u16* __restrict__ wqkv, const u16* __restrict__ wo,
                                                            const u16* __restrict__ w1, const u16* __restrict__ w2, u16* __restrict__ out, char* ws) {
    extern __shared__ __attribute__((aligned(16))) char smem_[];
    LAS char* sm = (LAS char*)smem_;
    const int bid = blockIdx.x, tid = threadIdx.x;
    const int w = __builtin_amdgcn_readfirstlane(tid >> 6), l = tid & 63;
    const int g = l >> 4, i16 = l & 15;
    const int xcd = bid & 7, idx = bid >> 3;
    const int rt = xcd * 4 + (idx >> 3), mem = idx & 7;
    STAMP(0);
    const auto rs_x = __builtin_amdgcn_make_buffer_rsrc((void*)x, 0, S_ * D_ * 2, 0x00020000);
    const auto rs_wqkv = __builtin_amdgcn_make_buffer_rsrc((void*)wqkv, 0, D_ * 3 * D_ * 2, 0x00020000);
    const auto rs_ws = __builtin_amdgcn_make_buffer_rsrc((void*)ws, 0, WS_USED, 0x00020000);
    unsigned tag = 0;
    if (w == 0) tag = __hip_atomic_load((gu32*)(ws + WS_EPOCH), RLX_AGENT);

    const int hg = mem, r0 = rt * 64;
    const int crot = (2 * (rt & 3) + hg) & 7;
    const int row8 = l >> 3, ch = l & 7;
    u32x4 sa[8], sb[8][3], sx[2];
    {
        #pragma unroll
        for (int e = 0; e < 2; e++)
            sx[e] = __builtin_amdgcn_raw_buffer_load_b128(rs_x, ((r0 - 16 + 64 + e * 8 + row8) * 512 + w * 64 + ch * 8) * 2, 0, 0);
    }
    int arow = r0 - 16 + w * 8 + row8; arow = arow < 0 ? 0 : arow;
    const unsigned a_off = (arow * 512 + ch * 8) * 2;
    const unsigned b_off = ((w * 8 + row8) * 1536 + hg * 64 + ch * 8) * 2;
    auto issue_chunk = [&](int c) {
        const int cc = (c + crot) & 7;
        sa[c] = __builtin_amdgcn_raw_buffer_load_b128(rs_x, a_off, cc * 128, 0);
        #pragma unroll
        for (int s = 0; s < 3; s++) sb[c][s] = __builtin_amdgcn_raw_buffer_load_b128(rs_wqkv, b_off + s * 1024, cc * (64 * 1536 * 2), 0);
    };
    constexpr int DEPTH = 2;
    #pragma unroll
    for (int c = 0; c < DEPTH; c++) issue_chunk(c);
    fence();
    const int task = bid * 8 + w;
    const u16* tsrc; unsigned tN, tkb, tcb, tdst, tnks;
    if (task < 256) { tsrc = wo; tN = 512; tkb = task >> 3; tcb = task & 7; tdst = WS_WOT; tnks = 16; }
    else if (task < 256 + 704) { int t = task - 256; tsrc = w1; tN = DFF; tkb = t / 22; tcb = t - tkb * 22; tdst = WS_W1T; tnks = 16; }
    else { int t = task - 960; t = t < 688 ? t : 687; tsrc = w2; tN = 512; tkb = t >> 3; tcb = t & 7; tdst = WS_W2T; tnks = 43; }
    const bool has_task = task < 1648;
    u32x4 pt[2];
    {
        const auto rs_t = __builtin_amdgcn_make_buffer_rsrc((void*)tsrc, 0, (tsrc == wo ? 512 * 512 : 512 * DFF) * 2, 0x00020000);
        unsigned col = tcb * 64 + ch * 8; col = col < tN ? col : 0;
        #pragma unroll
        for (int e = 0; e < 2; e++) pt[e] = __builtin_amdgcn_raw_buffer_load_b128(rs_t, ((tkb * 16 + e * 8 + row8) * tN + col) * 2, 0, 0);
    }
    fence();
    const int hh = w & 3, half = w >> 2;
    const unsigned a_wr = L_A + (w * 8 + row8) * 1024;
    const unsigned a_wr_x = ((w & 1) << 3 | row8);
    const unsigned sw_b = (((row8 >> 1) & 1) | ((w & 1) << 1)) << 1;
    const unsigned b_wr = L_B + (w * 8 + row8) * 128 + ((ch ^ sw_b) * 16);
    const int q4 = i16 >> 2, p4 = l & 3;
    const unsigned sw_r = ((q4 >> 1) | ((g & 1) << 1)) << 1;
    const unsigned b_rd = L_B + (8 * g + q4) * 128 + (((2 * hh + (p4 >> 1)) ^ sw_r) * 16) + (p4 & 1) * 8;
    const unsigned a_rd = L_A + (half * 32 + i16) * 1024;
    f32x4 accQ[3] = {}, accK[3] = {}, accV[3] = {};
    float st_s = 0.f, st_q = 0.f;
    const unsigned ONES = 0x3f803f80u;
    #pragma unroll
    for (int c = 0; c < 8; c++) {
        const int cc = (c + crot) & 7;
        if (c == 0) {
            #pragma unroll
            for (int e = 0; e < 2; e++) {
                lds_w128(sm + L_A + (64 + e * 8 + row8) * 1024 + (((8 * w + ch) ^ (e * 8 + row8)) * 16), sx[e]);
                float s = 0.f, q = 0.f;
                #pragma unroll
                for (int j = 0; j < 4; j++) { s = dot2(sx[e][j], ONES, s); q = dot2(sx[e][j], sx[e][j], q); }
                s = dpp_add<0xB1>(s); q = dpp_add<0xB1>(q); s = dpp_add<0x4E>(s); q = dpp_add<0x4E>(q); s = dpp_add<0x141>(s); q = dpp_add<0x141>(q);
                if (ch == 0) { f32x2 v; v[0] = s; v[1] = q; *(LAS f32x2*)(sm + L_XP + ((e * 8 + row8) * 8 + w) * 8) = v; }
            }
        }
        {
            u32x4 av = sa[c];
            #pragma unroll
            for (int j = 0; j < 4; j++) { st_s = dot2(av[j], ONES, st_s); st_q = dot2(av[j], av[j], st_q); }
            if (w == 0 && row8 == 0) { av[0] = ONES; av[1] = ONES; av[2] = ONES; av[3] = ONES; }
            lds_w128(sm + a_wr + (((8 * cc + ch) ^ a_wr_x) * 16), av);
            #pragma unroll
            for (int s = 0; s < 3; s++) lds_w128(sm + b_wr + (c & 1) * 24576 + s * 8192, sb[c][s]);
        }
        if (c == 7) {
            float s = st_s, q = st_q;
            s = dpp_add<0xB1>(s); q = dpp_add<0xB1>(q); s = dpp_add<0x4E>(s); q = dpp_add<0x4E>(q); s = dpp_add<0x141>(s); q = dpp_add<0x141>(q);
            if (ch == 0) *(LAS f32x2*)(sm + L_ST + (w * 8 + row8) * 8) = ln_stats(s, q);
        }
        fence();
        if (c + DEPTH < 8) issue_chunk(c + DEPTH);
        fence();
        if (c == 1) {
            LAS char* pp = sm + L_PT + w * 2048;
            #pragma unroll
            for (int e = 0; e < 2; e++) lds_w128(pp + (e * 8 + row8) * 128 + ((ch ^ ((((row8 >> 1) & 1) | (e << 1)) << 1)) * 16), pt[e]);
            const int tl = g >> 1, gg = g & 1;
            #pragma unroll
            for (int h2 = 0; h2 < 2; h2++) {
                const int t4 = h2 * 2 + tl;
                LAS char* rp = pp + (8 * gg + q4) * 128 + (((2 * t4 + (p4 >> 1)) ^ sw_r) * 16) + (p4 & 1) * 8;
                u32x2 lo = tr_read(rp), hi = tr_read(rp + 512);
                u32x4 v; v[0] = lo[0]; v[1] = lo[1]; v[2] = hi[0]; v[3] = hi[1];
                const unsigned nt = tcb * 4 + t4;
                unsigned off = tdst + ((nt * tnks + (tkb >> 1)) * 64 + ((2 * (tkb & 1) + gg) * 16 + i16)) * 16;
                if (!has_task || nt * 16 >= tN) off = 0x80000000u;
                __builtin_amdgcn_raw_buffer_store_b128(v, rs_ws, off, 0, 16);
            }
            fence();
        }
        bar();
        if (c == 1 && w == 7 && l < 16) {
            float s = 0.f, q = 0.f;
            #pragma unroll
            for (int j = 0; j < 8; j++) { f32x2 v = *(LAS f32x2*)(sm + L_XP + (l * 8 + j) * 8); s += v[0]; q += v[1]; }
            *(LAS f32x2*)(sm + L_ST + (64 + l) * 8) = ln_stats(s, q);
        }
        if (c == 5 && w == 0) {
            tag = __builtin_amdgcn_readfirstlane(tag) + 1u;
            if (l == 0) __hip_atomic_store((gu32*)(ws + WS_GFLAG + bid * 4), tag, RLX_AGENT);
        }
        #pragma unroll
        for (int ks = 0; ks < 2; ks++) {
            u32x4 af[3], bf[3];
            const unsigned qx = ((8 * cc + 4 * ks + g) ^ i16) * 16;
            #pragma unroll
            for (int a = 0; a < 3; a++) af[a] = lds_r128(sm + a_rd + a * 16384 + qx);
            #pragma unroll
            for (int s = 0; s < 3; s++) {
                LAS char* rp = sm + b_rd + (c & 1) * 24576 + s * 8192 + ks * 4096;
                u32x2 lo = tr_read(rp), hi = tr_read(rp + 512);
                bf[s][0] = lo[0]; bf[s][1] = lo[1]; bf[s][2] = hi[0]; bf[s][3] = hi[1];
            }
            #pragma unroll
            for (int a = 0; a < 3; a++) {
                accQ[a] = mfma32(bf[0], af[a], accQ[a]);
                accK[a] = mfma32(bf[1], af[a], accK[a]);
                accV[a] = mfma32(af[a], bf[2], accV[a]);
            }
        }
    }
    STAMP(1);
    if (half == 0) {
        LAS float* cs = (LAS float*)(sm + L_CS) + hh * 48;
        if (i16 == 0) { *(LAS f32x4*)(cs + 4 * g) = accQ[0]; *(LAS f32x4*)(cs + 16 + 4 * g) = accK[0]; }
        if (g == 0) cs[32 + i16] = accV[0][0];
    }
    bar();
    {
        LAS float* cs = (LAS float*)(sm + L_CS) + hh * 48;
        const f32x4 cq = *(LAS f32x4*)(cs + 4 * g), ck = *(LAS f32x4*)(cs + 16 + 4 * g);
        const float cv = cs[32 + i16];
        u32x2 qp[3], kp[3], vp[3];
        #pragma unroll
        for (int a = 0; a < 3; a++) {
            const f32x2 st = *(LAS f32x2*)(sm + L_ST + ((half * 2 + a) * 16 + i16) * 8);
            const f32x4 s01 = *(LAS f32x4*)(sm + L_ST + ((half * 2 + a) * 16 + 4 * g) * 8);
            const f32x4 s23 = *(LAS f32x4*)(sm + L_ST + ((half * 2 + a) * 16 + 4 * g + 2) * 8);
            float t[4];
            if (a > 0) {
                #pragma unroll
                for (int r = 0; r < 4; r++) t[r] = (accQ[a][r] - st[0] * cq[r]) * st[1];
                qp[a][0] = pk_bf16(t[0], t[1]); qp[a][1] = pk_bf16(t[2], t[3]);
            }
            #pragma unroll
            for (int r = 0; r < 4; r++) t[r] = (accK[a][r] - st[0] * ck[r]) * st[1];
            kp[a][0] = pk_bf16(t[0], t[1]); kp[a][1] = pk_bf16(t[2], t[3]);
            t[0] = (accV[a][0] - s01[0] * cv) * s01[1]; t[1] = (accV[a][1] - s01[2] * cv) * s01[3];
            t[2] = (accV[a][2] - s23[0] * cv) * s23[1]; t[3] = (accV[a][3] - s23[2] * cv) * s23[3];
            vp[a][0] = pk_bf16(t[0], t[1]); vp[a][1] = pk_bf16(t[2], t[3]);
        }
        const float NEG = -1e30f, CS = 0.25f * 1.44269504088896f;
        const int head = hg * 4 + hh;
        #pragma unroll
        for (int a = 1; a < 3; a++) {
            f32x4 z = {};
            f32x4 s1 = mfma16(kp[a - 1], qp[a], z), s2 = mfma16(kp[a], qp[a], z);
            const bool first = (rt == 0) && (half == 0) && (a == 1);
            float e[8];
            #pragma unroll
            for (int r = 0; r < 4; r++) {
                const int j = 4 * g + r; const bool par = ((i16 ^ j) & 1) == 0;
                e[r] = (par && j >= i16 + 2 && !first) ? s1[r] : NEG;
                e[4 + r] = (par && j <= i16) ? s2[r] : NEG;
            }
            float mx = fmaxf(fmaxf(fmaxf(e[0], e[1]), fmaxf(e[2], e[3])), fmaxf(fmaxf(e[4], e[5]), fmaxf(e[6], e[7])));
            mx = xmax16_32(mx);
            float sum = 0.f;
            #pragma unroll
            for (int r = 0; r < 8; r++) { e[r] = __builtin_amdgcn_exp2f((e[r] - mx) * CS); sum += e[r]; }
            sum = xsum16_32(sum);
            const float inv = 1.0f / sum;
            u32x4 pf, vf;
            pf[0] = pk_bf16(e[0] * inv, e[1] * inv); pf[1] = pk_bf16(e[2] * inv, e[3] * inv);
            pf[2] = pk_bf16(e[4] * inv, e[5] * inv); pf[3] = pk_bf16(e[6] * inv, e[7] * inv);
            vf[0] = vp[a - 1][0]; vf[1] = vp[a - 1][1]; vf[2] = vp[a][0]; vf[3] = vp[a][1];
            f32x4 o = mfma32(vf, pf, z);
            u32x2 op; op[0] = pk_bf16(o[0], o[1]); op[1] = pk_bf16(o[2], o[3]);
            const int tt = rt * 4 + half * 2 + (a - 1);
            const unsigned off = WS_OT + ((tt * 16 + (head >> 1)) * 64 + (2 * (head & 1) + (g >> 1)) * 16 + i16) * 16 + (g & 1) * 8;
            __builtin_amdgcn_raw_buffer_store_b64(op, rs_ws, off, 0, 16);
        }
    }
    STAMP(2);
    auto publish = [&](int seam) {
        drain();
        bar();
        if (tid == 0) __hip_atomic_store((gu32*)(ws + WS_SFLAG + (seam * 32 + rt) * 256 + mem * 4), tag, RLX_AGENT);
    };
    auto wait_group = [&](int seam) {
        if (w == 0) {
            for (unsigned spins = 0; spins < SPIN_CAP; spins++) {
                unsigned v = __hip_atomic_load((gu32*)(ws + WS_SFLAG + (seam * 32 + rt) * 256 + (l & 7) * 4), RLX_AGENT);
                if (__all(v == tag)) break;
                __builtin_amdgcn_s_sleep(1);
            }
        }
        bar();
    };
    publish(0);
    if (w == 0) {
        for (unsigned spins = 0; spins < SPIN_CAP; spins++) {
            u32x4 v = __builtin_amdgcn_raw_buffer_load_b128(rs_ws, WS_GFLAG + l * 16, 0, 16);
            if (__all(v[0] == tag && v[1] == tag && v[2] == tag && v[3] == tag)) break;
            __builtin_amdgcn_s_sleep(1);
        }
        if (bid == 0 && l == 0) __hip_atomic_store((gu32*)(ws + WS_EPOCH), tag, RLX_AGENT);
    }
    bar();
    STAMP(3);
    const int rh = mem >> 2, cq = mem & 3;
    const int ntg = cq * 8 + w;
    const int tt0 = rt * 4 + rh * 2;
    u32x4 bo[16];
    u32x2 xr[2];
    if (w != 0) {
        #pragma unroll
        for (int ks = 0; ks < 16; ks++) bo[ks] = __builtin_amdgcn_raw_buffer_load_b128(rs_ws, WS_WOT + (ntg * 16 + ks) * 1024 + l * 16, 0, 16);
    }
    wait_group(0);
    STAMP(4);
    {
        u32x4 sg[4];
        #pragma unroll
        for (int i = 0; i < 4; i++) { const int p = w + 8 * i; sg[i] = __builtin_amdgcn_raw_buffer_load_b128(rs_ws, WS_OT + ((tt0 + (p & 1)) * 16 + (p >> 1)) * 1024 + l * 16, 0, 16); }
        if (w == 0) {
            #pragma unroll
            for (int ks = 0; ks < 16; ks++) bo[ks] = __builtin_amdgcn_raw_buffer_load_b128(rs_ws, WS_WOT + (ntg * 16 + ks) * 1024 + l * 16, 0, 16);
        }
        #pragma unroll
        for (int m = 0; m < 2; m++) xr[m] = __builtin_amdgcn_raw_buffer_load_b64(rs_x, (((tt0 + m) * 16 + i16) * 512 + ntg * 16 + 4 * g) * 2, 0, 0);
        fence();
        #pragma unroll
        for (int i = 0; i < 4; i++) lds_w128(sm + (w + 8 * i) * 1024 + l * 16, sg[i]);
    }
    bar();
    STAMP(5);
    u32x2 hres[2];
    {
        f32x4 acc[2] = {};
        #pragma unroll
        for (int ks = 0; ks < 16; ks++) {
            #pragma unroll
            for (int m = 0; m < 2; m++) acc[m] = mfma32(bo[ks], lds_r128(sm + (ks * 2 + m) * 1024 + l * 16), acc[m]);
        }
        #pragma unroll
        for (int m = 0; m < 2; m++) {
            hres[m][0] = pk_bf16(bf_lo(xr[m][0]) + rbf(acc[m][0]), bf_hi(xr[m][0]) + rbf(acc[m][1]));
            hres[m][1] = pk_bf16(bf_lo(xr[m][1]) + rbf(acc[m][2]), bf_hi(xr[m][1]) + rbf(acc[m][3]));
            const unsigned off = WS_HT + (((tt0 + m) * 16 + (ntg >> 1)) * 64 + (2 * (ntg & 1) + (g >> 1)) * 16 + i16) * 16 + (g & 1) * 8;
            __builtin_amdgcn_raw_buffer_store_b64(hres[m], rs_ws, off, 0, 16);
        }
    }
    STAMP(6);
    publish(1);
    const int tbase = mem < 6 ? mem * 11 : 66 + (mem - 6) * 10, tcnt = mem < 6 ? 11 : 10;
    const int kh = w >> 2;
    const int T1 = tbase + w;
    const bool v2 = 8 + (w & 3) < tcnt;
    const int T2 = v2 ? tbase + 8 + (w & 3) : tbase;
    u32x4 b1[16], b2[8];
    auto load_w1 = [&]() {
        #pragma unroll
        for (int k = 0; k < 16; k++) {
            b1[k] = __builtin_amdgcn_raw_buffer_load_b128(rs_ws, WS_W1T + (T1 * 16 + (k ^ (kh * 8))) * 1024 + l * 16, 0, 16);
            if (k < 8) b2[k] = __builtin_amdgcn_raw_buffer_load_b128(rs_ws, WS_W1T + (T2 * 16 + kh * 8 + k) * 1024 + l * 16, 0, 16);
        }
    };
    if (w != 0) load_w1();
    wait_group(1);
    STAMP(7);
    f32x4 c1 = {}, c2 = {};
    {
        u32x4 sg[8];
        #pragma unroll
        for (int i = 0; i < 8; i++) sg[i] = __builtin_amdgcn_raw_buffer_load_b128(rs_ws, WS_HT + ((rt * 4 + (w & 3)) * 16 + kh * 8 + i) * 1024 + l * 16, 0, 16);
        if (w == 0) load_w1();
        fence();
        float s = 0.f, q = 0.f;
        #pragma unroll
        for (int i = 0; i < 8; i++) {
            lds_w128(sm + ((kh * 8 + i) * 4 + (w & 3)) * 1024 + l * 16, sg[i]);
            #pragma unroll
            for (int j = 0; j < 4; j++) { s = dot2(sg[i][j], ONES, s); q = dot2(sg[i][j], sg[i][j], q); }
        }
        s = xsum16_32(s); q = xsum16_32(q);
        if (g == 0) { f32x2 v; v[0] = s; v[1] = q; *(LAS f32x2*)(sm + L_S3 + (kh * 64 + (w & 3) * 16 + i16) * 8) = v; }
    }
    bar();
    STAMP(8);
    {
        f32x4 a1[4] = {}, a2[4] = {};
        u32x4 ones; ones[0] = ONES; ones[1] = ONES; ones[2] = ONES; ones[3] = ONES;
        #pragma unroll
        for (int k = 0; k < 16; k++) {
            u32x4 af[4];
            #pragma unroll
            for (int m = 0; m < 4; m++) af[m] = lds_r128(sm + (((k ^ (kh * 8))) * 4 + m) * 1024 + l * 16);
            #pragma unroll
            for (int m = 0; m < 4; m++) a1[m] = mfma32(b1[k], af[m], a1[m]);
            c1 = mfma32(b1[k], ones, c1);
            if (k < 8) {
                #pragma unroll
                for (int m = 0; m < 4; m++) a2[m] = mfma32(b2[k], af[m], a2[m]);
                c2 = mfma32(b2[k], ones, c2);
            }
        }
        LAS char* xo = sm + L_X3 + w * 3072 + l * 16;
        *(LAS f32x4*)(xo) = kh ? a2[0] : a2[2];
        *(LAS f32x4*)(xo + 1024) = kh ? a2[1] : a2[3];
        *(LAS f32x4*)(xo + 2048) = c2;
        f32x2 st[4];
        #pragma unroll
        for (int m = 0; m < 4; m++) {
            const f32x2 u = *(LAS f32x2*)(sm + L_S3 + (m * 16 + i16) * 8), v = *(LAS f32x2*)(sm + L_S3 + (64 + m * 16 + i16) * 8);
            st[m] = ln_stats(u[0] + v[0], u[1] + v[1]);
        }
        auto put_f = [&](f32x4 a, f32x4 c, f32x2 s, int m, int T, bool ok) {
            float t[4];
            #pragma unroll
            for (int r = 0; r < 4; r++) t[r] = fmaxf((a[r] - s[0] * c[r]) * s[1], 0.f);
            u32x2 o; o[0] = pk_bf16(t[0], t[1]); o[1] = pk_bf16(t[2], t[3]);
            unsigned off = WS_FT + (((rt * 4 + m) * 43 + (T >> 1)) * 64 + (2 * (T & 1) + (g >> 1)) * 16 + i16) * 16 + (g & 1) * 8;
            if (!ok) off = 0x80000000u;
            __builtin_amdgcn_raw_buffer_store_b64(o, rs_ws, off, 0, 16);
        };
        #pragma unroll
        for (int m = 0; m < 4; m++) put_f(a1[m], c1, st[m], m, T1, true);
        bar();
        LAS char* xi = sm + L_X3 + (w ^ 4) * 3072 + l * 16;
        const f32x4 p0 = *(LAS f32x4*)(xi), p1 = *(LAS f32x4*)(xi + 1024), pc = *(LAS f32x4*)(xi + 2048);
        const f32x4 cc2 = c2 + pc;
        put_f((kh ? a2[2] : a2[0]) + p0, cc2, kh ? st[2] : st[0], 2 * kh, T2, v2);
        put_f((kh ? a2[3] : a2[1]) + p1, cc2, kh ? st[3] : st[1], 2 * kh + 1, T2, v2);
    }
    STAMP(9);
    publish(2);
    constexpr int NPRE = 32;
    u32x4 bw[43];
    auto load_w2a = [&]() {
        #pragma unroll
        for (int ks = 0; ks < NPRE; ks++) bw[ks] = __builtin_amdgcn_raw_buffer_load_b128(rs_ws, WS_W2T + (ntg * 43 + ks) * 1024 + l * 16, 0, 16);
    };
    if (w != 0) load_w2a();
    wait_group(2);
    STAMP(10);
    {
        u32x4 sg[11];
        #pragma unroll
        for (int i = 0; i < 11; i++) {
            const int p = w + 8 * i;
            unsigned off = WS_FT + ((tt0 + (p & 1)) * 43 + (p >> 1)) * 1024 + l * 16;
            if (p >= 86) off = 0x80000000u;
            sg[i] = __builtin_amdgcn_raw_buffer_load_b128(rs_ws, off, 0, 16);
        }
        if (w == 0) load_w2a();
        fence();
        #pragma unroll
        for (int i = 0; i < 11; i++) lds_w128(sm + (w + 8 * i) * 1024 + l * 16, sg[i]);
        fence();
        #pragma unroll
        for (int ks = NPRE; ks < 43; ks++) bw[ks] = __builtin_amdgcn_raw_buffer_load_b128(rs_ws, WS_W2T + (ntg * 43 + ks) * 1024 + l * 16, 0, 16);
    }
    bar();
    STAMP(11);
    {
        f32x4 acc[2] = {};
        #pragma unroll
        for (int ks = 0; ks < 43; ks++) {
            #pragma unroll
            for (int m = 0; m < 2; m++) acc[m] = mfma32(bw[ks], lds_r128(sm + (ks * 2 + m) * 1024 + l * 16), acc[m]);
        }
        #pragma unroll
        for (int m = 0; m < 2; m++) {
            u32x2 o;
            o[0] = pk_bf16(bf_lo(hres[m][0]) + rbf(acc[m][0]), bf_hi(hres[m][0]) + rbf(acc[m][1]));
            o[1] = pk_bf16(bf_lo(hres[m][1]) + rbf(acc[m][2]), bf_hi(hres[m][1]) + rbf(acc[m][3]));
            *(u32x2*)(out + ((tt0 + m) * 16 + i16) * 512 + ntg * 16 + 4 * g) = o;
        }
    }
    STAMP(12);
}

extern "C" void kernel_launch(void* const* d_in, const int* in_sizes, int n_in, void* d_out, int out_size, void* d_ws, size_t ws_size, hipStream_t stream) {
    (void)in_sizes; (void)n_in; (void)out_size; (void)ws_size;
    (void)hipFuncSetAttribute((const void*)fused_block_kernel, hipFuncAttributeMaxDynamicSharedMemorySize, LDS_BYTES);
    fused_block_kernel<<<256, 512, LDS_BYTES, stream>>>((const u16*)d_in[0], (const u16*)d_in[1], (const u16*)d_in[2], (const u16*)d_in[4], (const u16*)d_in[5], (u16*)d_out, (char*)d_ws);
}
```

```cpp
#include <hip/hip_runtime.h>
#include <hip/hip_bf16.h>
#include <stdint.h>

typedef unsigned short u16;
typedef unsigned long long u64;
typedef __attribute__((ext_vector_type(8))) short s16x8;
typedef __attribute__((ext_vector_type(4))) short s16x4;
typedef __attribute__((ext_vector_type(4))) float f32x4;
typedef __attribute__((ext_vector_type(2))) float f32x2;
typedef __attribute__((ext_vector_type(4))) unsigned u32x4;
typedef __attribute__((ext_vector_type(2))) unsigned u32x2;
typedef __attribute__((ext_vector_type(2))) __bf16 bf16x2;
#define GAS __attribute__((address_space(1)))
#define LAS __attribute__((address_space(3)))
typedef GAS unsigned gu32;
#define RLX_AGENT __ATOMIC_RELAXED, __HIP_MEMORY_SCOPE_AGENT
#define DEV __device__ __forceinline__

#define SLP 1
#define ABL 0
namespace {
constexpr int S_ = 2048, D_ = 512, DFF = 1376;
constexpr unsigned WS_EPOCH = 0;
constexpr unsigned WS_SFLAG = 8192;
constexpr unsigned WS_ST1 = 131072;
constexpr unsigned WS_ST2 = 262144;
constexpr unsigned WS_DIAG  = 65536;
constexpr unsigned WS_OT  = 1u << 20;
constexpr unsigned WS_HT  = 3u << 20;
constexpr unsigned WS_FT  = 5u << 20;
constexpr unsigned WS_USED = 11u << 20;
constexpr unsigned L_SLOT = 34816;
constexpr unsigned L_RING = 69632;
constexpr unsigned L_XCH = 143360;
constexpr unsigned L_CTL = 159744;
constexpr unsigned L_ST = L_CTL + 256;
constexpr unsigned L_S3 = L_CTL + 1024;
constexpr unsigned L_DG = L_CTL + 2048;
constexpr unsigned LDS_BYTES = 163840;
constexpr unsigned SPIN_CAP = 1u << 15;
constexpr int NA = 18;

DEV float bf_lo(unsigned v) { return __uint_as_float(v << 16); }
DEV float bf_hi(unsigned v) { return __uint_as_float(v & 0xffff0000u); }
DEV unsigned pk_bf16(float a, float b) { bf16x2 r; r[0] = (__bf16)a; r[1] = (__bf16)b; return __builtin_bit_cast(unsigned, r); }
DEV float rbf(float a) { return (float)((__bf16)a); }
DEV float dot2(unsigned a, unsigned b, float c) {
    return __builtin_amdgcn_fdot2_f32_bf16(__builtin_bit_cast(bf16x2, a), __builtin_bit_cast(bf16x2, b), c, false);
}
DEV f32x4 mfma32(u32x4 a, u32x4 b, f32x4 c) {
    return __builtin_amdgcn_mfma_f32_16x16x32_bf16(__builtin_bit_cast(s16x8, a), __builtin_bit_cast(s16x8, b), c, 0, 0, 0);
}
DEV f32x4 mfma16(u32x2 a, u32x2 b, f32x4 c) {
    return __builtin_amdgcn_mfma_f32_16x16x16bf16_1k(__builtin_bit_cast(s16x4, a), __builtin_bit_cast(s16x4, b), c, 0, 0, 0);
}
DEV u32x2 tr_read(LAS char* p) { return __builtin_bit_cast(u32x2, __builtin_amdgcn_ds_read_tr16_b64_v4i16((LAS s16x4*)p)); }
DEV u32x4 tr_frag(LAS char* p) { u32x2 lo = tr_read(p), hi = tr_read(p + 512); u32x4 v; v[0] = lo[0]; v[1] = lo[1]; v[2] = hi[0]; v[3] = hi[1]; return v; }
DEV u32x4 lds_r128(LAS char* p) { return *(LAS u32x4*)p; }
template <int CTRL> DEV float dpp_add(float v) {
    return v + __uint_as_float(__builtin_amdgcn_update_dpp(0, __float_as_uint(v), CTRL, 0xf, 0xf, true));
}
DEV float xsum16_32(float v) {
    auto a = __builtin_amdgcn_permlane16_swap(__float_as_uint(v), __float_as_uint(v), false, false);
    v = __uint_as_float(a[0]) + __uint_as_float(a[1]);
    auto b = __builtin_amdgcn_permlane32_swap(__float_as_uint(v), __float_as_uint(v), false, false);
    return __uint_as_float(b[0]) + __uint_as_float(b[1]);
}
DEV float xmax16_32(float v) {
    auto a = __builtin_amdgcn_permlane16_swap(__float_as_uint(v), __float_as_uint(v), false, false);
    v = fmaxf(__uint_as_float(a[0]), __uint_as_float(a[1]));
    auto b = __builtin_amdgcn_permlane32_swap(__float_as_uint(v), __float_as_uint(v), false, false);
    return fmaxf(__uint_as_float(b[0]), __uint_as_float(b[1]));
}
DEV f32x2 ln_stats(float s, float q) {
    float m = s * (1.0f / 512.0f);
    float var = fmaxf(q * (1.0f / 512.0f) - m * m, 0.0f);
    float sd = rbf(__builtin_amdgcn_sqrtf(rbf(rbf(var) + 1e-5f)));
    f32x2 r; r[0] = rbf(m); r[1] = __builtin_amdgcn_rcpf(sd); return r;
}
DEV unsigned ln_pack(float s, float q) {
    float m = s * (1.0f / 512.0f);
    float var = fmaxf(q * (1.0f / 512.0f) - m * m, 0.0f);
    return pk_bf16(m, __builtin_amdgcn_sqrtf(rbf(rbf(var) + 1e-5f)));
}
typedef __attribute__((ext_vector_type(2))) short s16x2;
DEV unsigned relu_pk(unsigned v) { const s16x2 z = {0, 0}; return __builtin_bit_cast(unsigned, __builtin_elementwise_max(__builtin_bit_cast(s16x2, v), z)); }
DEV float sel(bool c, float a, float b) { return c ? a : b; }
DEV float vmax(float a, float b) { float r; asm("v_max_f32 %0, %1, %2" : "=v"(r) : "v"(a), "v"(b)); return r; }
DEV void fence() { asm volatile("" ::: "memory"); }
DEV void sfence() { asm volatile("" ::: "memory"); __builtin_amdgcn_sched_barrier(0); }
#define VMCNT(n) asm volatile("s_waitcnt vmcnt(" #n ")" ::: "memory")
#define SGB(mask, n) __builtin_amdgcn_sched_group_barrier(mask, n, 0)
#define PIN(x) asm volatile("" : "+v"(x))
#define RFL(x) ((unsigned)__builtin_amdgcn_readfirstlane(x))
#define DMA(rs, ldsoff, voff, soff, imm, aux) __builtin_amdgcn_raw_ptr_buffer_load_lds(rs, (LAS void*)(sm + (ldsoff)), 16, voff, soff, imm, aux)
#define DMA4(rs, ldsoff, vo, soff, aux) do { const unsigned lo_ = (ldsoff), so_ = (soff); DMA(rs, lo_, vo[0], so_, 0, aux); DMA(rs, lo_, vo[1], so_, 1024, aux); \
    DMA(rs, lo_, vo[2], so_, 2048, aux); DMA(rs, lo_, vo[3], so_, 3072, aux); } while (0)
DEV void bar() { asm volatile("s_waitcnt lgkmcnt(0)" ::: "memory"); __builtin_amdgcn_s_barrier(); asm volatile("" ::: "memory"); }
DEV void barx() { asm volatile("" ::: "memory"); __builtin_amdgcn_s_barrier(); asm volatile("" ::: "memory"); }
#define STAMP(i) do {} while (0)
}

__global__ __launch_bounds__(512, 2) void fused_block_kernel(const u16* __restrict__ x, const u16* __restrict__ wqkv, const u16* __restrict__ wo,
                                                            const u16* __restrict__ w1, const u16* __restrict__ w2, u16* __restrict__ out, char* ws) {
    extern __shared__ __attribute__((aligned(16))) char smem_[];
    LAS char* sm = (LAS char*)smem_;
    const int bid = blockIdx.x, tid = threadIdx.x;
    const int w = __builtin_amdgcn_readfirstlane(tid >> 6), l = tid & 63;
    const int g = l >> 4, i16 = l & 15;
    const int xcd = bid & 7, idx = bid >> 3;
    const int rt = xcd * 4 + (idx >> 3), mem = idx & 7;
    const int rt1 = (xcd >> 1) * 8 + (idx >> 2), hg = (xcd & 1) * 4 + (idx & 3);
    const int urot = (idx >> 3) * 2;
    const int crot = ((rt1 & 7) + 2 * (hg & 3)) & 7;
    const int r0 = rt1 * 64;
    const int row8 = l >> 3, ch = l & 7;
    const auto rs_x = __builtin_amdgcn_make_buffer_rsrc((void*)x, 0, S_ * D_ * 2, 0x00020000);
    const auto rs_ws = __builtin_amdgcn_make_buffer_rsrc((void*)ws, 0, WS_USED, 0x00020000);
    const unsigned OOB = 0x80000000u;
    const unsigned ONES = 0x3f803f80u;
    STAMP(0);

    if (w == 4 || w == 5) {
        const int lw = w - 4;
        const auto rs_wqkv = __builtin_amdgcn_make_buffer_rsrc((void*)wqkv, 0, D_ * 3 * D_ * 2, 0x00020000);
        unsigned a_vo[5], q_vo[4];
        #pragma unroll
        for (int e = 0; e < 5; e++) {
            int ar = r0 - 16 + (lw * 5 + e) * 8 + row8; ar = ar < 0 ? 0 : ar;
            a_vo[e] = (ar * 512 + ((ch ^ row8) * 8)) * 2 + 4096 - (e ? (e - 1) * 1024 : 0);
        }
        const auto rs_xm = __builtin_amdgcn_make_buffer_rsrc((void*)((const char*)x - 4096), 0, S_ * D_ * 2 + 4096, 0x00020000);
        const unsigned swz = (ch ^ ((((row8 >> 1) & 1) | (lw << 1)) << 1)) * 16;
        #pragma unroll
        for (int i = 0; i < 4; i++) q_vo[i] = (lw * 8 + i * 16 + row8) * 3072 + hg * 128 + swz - i * 1024;
        auto P1 = [&](int c) {
            const int cc = (c + crot) & 7;
            const unsigned sl = (c & 3) * L_SLOT;
            unsigned so = cc * 128; asm volatile("" : "+s"(so));
            DMA4(rs_xm, sl + lw * 5120 + 1024, (a_vo + 1), so, 0);
            if (lw == 1 || l >= 8) DMA(rs_xm, sl + lw * 5120, a_vo[0], so, 0, 0);
            fence();
            #pragma unroll
            for (int s = 0; s < 3; s++) DMA4(rs_wqkv, sl + 10240 + s * 8192 + lw * 4096, q_vo, cc * (64 * 3072) + s * 1024, 0);
        };
        P1(0); P1(1);
        VMCNT(17);
        barx();
        #pragma nounroll
        for (int r = 0; r < 6; r++) { P1(r + 2); VMCNT(17); barx(); }
        const auto rs_wo = __builtin_amdgcn_make_buffer_rsrc((void*)wo, 0, D_ * D_ * 2, 0x00020000);
        const auto rs_w1 = __builtin_amdgcn_make_buffer_rsrc((void*)w1, 0, D_ * DFF * 2, 0x00020000);
        const auto rs_w2 = __builtin_amdgcn_make_buffer_rsrc((void*)w2, 0, D_ * DFF * 2, 0x00020000);
        unsigned wo_vo[4], w1_vo[4], w2_vo[4], lin_vo[4], f_vo[4];
        #pragma unroll
        for (int i = 0; i < 4; i++) {
            wo_vo[i] = (lw * 8 + i * 16 + row8) * 1024 + mem * 128 + swz - i * 1024;
            w1_vo[i] = (lw * 8 + i * 16 + row8) * (DFF * 2) + mem * 384 + swz - i * 1024;
            w2_vo[i] = (lw * 8 + (i & 1) * 16 + (i >> 1) * 704 + row8) * 1024 + mem * 128 + swz - i * 1024;
            lin_vo[i] = l * 16;
            f_vo[i] = l * 16 + (i >> 1) * (20 * 1024);
        }
        auto WO = [&](int G) {
            #pragma unroll
            for (int i = 0; i < 4; i++) { const unsigned u = G * 4 + ((i + (urot >> 1)) & 3); DMA4(rs_wo, u * 8192 + lw * 4096, wo_vo, u * 65536, 0); }
        };
        auto W1 = [&](int G) {
            const unsigned u = (G + urot) & 7;
            #pragma unroll
            for (int s = 0; s < 3; s++) DMA4(rs_w1, L_RING + (G % 3) * 24576 + s * 8192 + lw * 4096, w1_vo, u * (64 * DFF * 2) + s * 128, 0);
        };
        auto W2 = [&](int G, int n) {
            #pragma unroll
            for (int u = 0; u < 3; u++) if (u < n) DMA4(rs_w2, L_RING + ((G + 2) % 3) * 24576 + u * 8192 + lw * 4096, w2_vo, (3 * G + u) * 32768, 0);
        };
        WO(0); VMCNT(16); barx();
        barx();
        WO(1); W1(0); W1(1); W1(2); VMCNT(12); barx();
        STAMP(1);
        barx();
        STAMP(2);
        {
            const unsigned src = WS_OT + (rt * 4 + lw) * 16384;
            #pragma unroll
            for (int hf = 0; hf < 2; hf++)
                #pragma unroll
                for (int i = 0; i < 4; i++) {
                    const unsigned o = (i >> 1) * 32768 + ((i & 1) * 4 + hf * 8) * 1024;
                    DMA4(rs_ws, lw * 16384 + o, lin_vo, src + o, 16);
                }
        }
        VMCNT(16); barx();
        STAMP(3);
        W1(3); VMCNT(12); barx();
        W1(4); VMCNT(0); barx();
        barx();
        W1(5); W1(6); W1(7);
        STAMP(4);
        barx();
        STAMP(5);
        {
            const unsigned src = WS_HT + (rt * 4 + lw) * 16384;
            #pragma unroll
            for (int p = 0; p < 8; p++) {
                const unsigned u = ((p == 0 ? 0 : p < 4 ? p + 4 : p - 3) + urot) & 7;
                #pragma unroll
                for (int m = 0; m < 2; m++) {
                    const unsigned o = m * 32768 + u * 2048;
                    DMA(rs_ws, lw * 16384 + o, lin_vo[0], src + o, 0, 16); DMA(rs_ws, lw * 16384 + o, lin_vo[0], src + o, 1024, 16);
                }
            }
        }
        VMCNT(24); barx();
        STAMP(6);
        VMCNT(16); barx();
        W2(0, 3); VMCNT(20); barx();
        W2(1, 3); W2(2, 3); VMCNT(36); barx();
        barx();
        VMCNT(0); barx();
        barx();
        W2(3, 3); W2(4, 3); W2(5, 3); VMCNT(0);
        STAMP(7);
        barx();
        STAMP(8);
        const unsigned fsrc = WS_FT + (rt * 4 + lw) * (43 * 1024);
        auto FG = [&](unsigned F) {
            #pragma unroll
            for (int m = 0; m < 2; m++) DMA4(rs_ws, (F & 3) * 16384 + (2 * m + lw) * 4096, f_vo, fsrc + m * (86 * 1024) + F * 2048, 16);
        };
        FG(0); FG(1); FG(2); FG(3);
        VMCNT(24); barx();
        STAMP(9);
        W2(6, 3); W2(7, 1);
        VMCNT(32); barx();
        FG(4); VMCNT(32); barx();
        FG(5); VMCNT(32); barx();
        #pragma nounroll
        for (unsigned F = 6; F < 10; F++) { FG(F); VMCNT(16); barx(); }
        f_vo[3] = OOB;
        FG(10); VMCNT(16); barx();
        VMCNT(8); barx();
        VMCNT(0); barx();
        STAMP(10);
        barx();
    } else if (w >= 6) {
        const int hw = w - 6;
        u32x4 xv[8];
        {
            int ar = r0 - 16 + hg * 10 + hw * 5 + row8; ar = ar < 0 ? 0 : ar;
            const unsigned vo = l < 40 ? (unsigned)(ar * 1024 + ch * 16) : OOB;
            #pragma unroll
            for (int i = 0; i < 8; i++) xv[i] = __builtin_amdgcn_raw_buffer_load_b128(rs_x, vo + i * 128, 0, 0);
        }
        unsigned tag = __hip_atomic_load((gu32*)(ws + WS_EPOCH + (l == 0 ? rt1 : rt) * 256), RLX_AGENT) + 1u;
        if (hw == 1 && l < 32) { u32x4 o; o[0] = ONES; o[1] = ONES; o[2] = ONES; o[3] = ONES; *(LAS u32x4*)(sm + (l >> 3) * L_SLOT + (l & 7) * 16) = o; }
        sfence();
        bar();
        float ss[2] = {}, qq[2] = {};
        auto st1 = [&](int i0, int i1) {
            #pragma unroll
            for (int i = i0; i < i1; i++)
                #pragma unroll
                for (int j = 0; j < 4; j++) { ss[j & 1] = dot2(xv[i][j], ONES, ss[j & 1]); qq[j & 1] = dot2(xv[i][j], xv[i][j], qq[j & 1]); }
            PIN(ss[0]); PIN(ss[1]); PIN(qq[0]); PIN(qq[1]);
            sfence();
        };
        st1(0, 3); barx();
        st1(3, 6); barx();
        st1(6, 8); barx();
        const unsigned tag1 = (unsigned)__builtin_amdgcn_readlane(tag, 0), tagr = (unsigned)__builtin_amdgcn_readlane(tag, 1);
        {
            float s = ss[0] + ss[1], q = qq[0] + qq[1];
            s = dpp_add<0xB1>(s); q = dpp_add<0xB1>(q); s = dpp_add<0x4E>(s); q = dpp_add<0x4E>(q); s = dpp_add<0x141>(s); q = dpp_add<0x141>(q);
            u32x2 gr; gr[0] = ln_pack(s, q); gr[1] = tag1;
            __builtin_amdgcn_raw_buffer_store_b64(gr, rs_ws, (ch == 0 && l < 40) ? WS_ST1 + (rt1 * 80 + hg * 10 + hw * 5 + row8) * 8 : OOB, 0, 16);
        }
        sfence();
        barx(); barx();
        const unsigned g1_off = l < 40 ? WS_ST1 + (rt1 * 80 + hw * 40 + l) * 8 : OOB;
        u32x2 gr = __builtin_amdgcn_raw_buffer_load_b64(rs_ws, g1_off, 0, 16);
        sfence();
        barx();
        #pragma nounroll
        for (unsigned s = 0; s < SPIN_CAP; s++) {
            if (__all(l >= 40 || gr[1] == tag1)) break;
            __builtin_amdgcn_s_sleep(1);
            gr = __builtin_amdgcn_raw_buffer_load_b64(rs_ws, g1_off, 0, 16);
        }
        if (l < 40) { f32x2 v; v[0] = bf_lo(gr[0]); v[1] = __builtin_amdgcn_rcpf(bf_hi(gr[0])); *(LAS f32x2*)(sm + L_ST + (hw * 40 + l) * 8) = v; }
        STAMP(1);
        bar();
        barx();
        auto signal = [&](int seam, int grp, int slot, unsigned tg) {
            if (hw == 0 && l == 0) __hip_atomic_store((gu32*)(ws + WS_SFLAG + (seam * 32 + grp) * 256 + slot * 4), tg, RLX_AGENT);
        };
        auto poll = [&](int seam) {
            if (hw == 0) {
                #pragma nounroll
                for (unsigned s = 0; s < SPIN_CAP; s++) {
                    const unsigned v = __hip_atomic_load((gu32*)(ws + WS_SFLAG + (seam * 32 + rt) * 256 + (l & 7) * 4), RLX_AGENT);
                    if (__all(v == tagr)) break;
                    __builtin_amdgcn_s_sleep(1);
                }
            }
        };
        barx();
        signal(0, rt1, hg, tag1);
        poll(0);
        STAMP(2);
        barx(); barx(); barx();
        barx();
        signal(1, rt, mem, tagr);
        {
            u32x4 v[2][2];
            #pragma unroll
            for (int m = 0; m < 2; m++)
                #pragma unroll
                for (int n = 0; n < 2; n++) v[m][n] = lds_r128(sm + L_XCH + (hw * 2 + n) * 4096 + m * 1024 + l * 16);
            bar();
            float s2[2] = {}, q2[2] = {};
            #pragma unroll
            for (int n = 0; n < 2; n++)
                #pragma unroll
                for (int j = 0; j < 4; j++)
                    #pragma unroll
                    for (int m = 0; m < 2; m++) { s2[m] = dot2(v[m][n][j], ONES, s2[m]); q2[m] = dot2(v[m][n][j], v[m][n][j], q2[m]); }
            const float sa = xsum16_32(s2[0]), qa = xsum16_32(q2[0]), sb = xsum16_32(s2[1]), qb = xsum16_32(q2[1]);
            u32x4 o; o[0] = __float_as_uint(g ? sb : sa); o[1] = tagr; o[2] = __float_as_uint(g ? qb : qa); o[3] = tagr;
            __builtin_amdgcn_raw_buffer_store_b128(o, rs_ws, l < 32 ? WS_ST2 + ((rt * 8 + mem) * 64 + hw * 32 + l) * 16 : OOB, 0, 16);
            sfence();
        }
        poll(1);
        STAMP(3);
        barx();
        if (hw == 0 && mem == 0 && l == 0) __hip_atomic_store((gu32*)(ws + WS_EPOCH + rt * 256), tagr, RLX_AGENT);
        barx(); barx();
        if (hw == 1) {
            #pragma nounroll
            for (unsigned s = 0; s < SPIN_CAP; s++) {
                u32x4 p[8];
                #pragma unroll
                for (int i = 0; i < 8; i++) p[i] = __builtin_amdgcn_raw_buffer_load_b128(rs_ws, WS_ST2 + (rt * 512 + l) * 16 + i * 1024, 0, 16);
                bool ok = true; float sx = 0.f, qx = 0.f;
                #pragma unroll
                for (int i = 0; i < 8; i++) { ok = ok && p[i][1] == tagr && p[i][3] == tagr; sx += __uint_as_float(p[i][0]); qx += __uint_as_float(p[i][2]); }
                *(LAS f32x2*)(sm + L_S3 + l * 8) = ln_stats(sx, qx);
                if (__all(ok)) break;
                __builtin_amdgcn_s_sleep(1);
            }
        }
        STAMP(4);
        bar(); barx(); barx();
        barx();
        signal(2, rt, mem, tagr);
        barx();
        poll(2);
        STAMP(5);
        #pragma nounroll
        for (int i = 24; i <= 36; i++) barx();
    } else {
        const int j = w;
        const int n2 = j & 1, kh = j >> 1;
        const int q4 = i16 >> 2, p4 = l & 3;
        const unsigned sw_r = ((q4 >> 1) | ((g & 1) << 1)) << 1;
        const unsigned tr_row = ((g & 1) * 4 + (g >> 1)) * 1024 + q4 * 128;
        const unsigned tr_plain = tr_row + (((2 * j + (p4 >> 1)) ^ sw_r) * 16) + (p4 & 1) * 8;
        const unsigned tr_pair = tr_row + (((4 * n2 + p4) ^ sw_r) * 16);
        const int cb32 = mem * 2 + n2;
        u32x4 xr[2];
        #pragma unroll
        for (int m = 0; m < 2; m++) xr[m] = __builtin_amdgcn_raw_buffer_load_b128(rs_x, (((rt * 4 + 2 * kh + m) * 16 + i16) * 512 + cb32 * 32 + 8 * g) * 2, 0, 0);
        {
            const unsigned a_rd0 = i16 * 128 + ((g ^ (i16 & 7)) * 16);
            const unsigned a_rd1 = a_rd0 ^ 64;
            f32x4 accQ[5] = {}, accK[5] = {}, accV[5] = {};
            u32x4 af[2][5], bf[2][3];
            auto R = [&](int s) {
                LAS char* sl = sm + ((s >> 1) & 3) * L_SLOT;
                #pragma unroll
                for (int t = 0; t < 3; t++) bf[s & 1][t] = tr_frag(sl + 10240 + tr_plain + t * 8192 + (s & 1) * 2048);
                #pragma unroll
                for (int t = 0; t < 5; t++) af[s & 1][t] = lds_r128(sl + ((s & 1) ? a_rd1 : a_rd0) + t * 2048);
            };
            auto M = [&](int s) {
                #pragma unroll
                for (int a = 0; a < 5; a++) {
                    accQ[a] = mfma32(bf[s & 1][0], af[s & 1][a], accQ[a]);
                    accK[a] = mfma32(bf[s & 1][1], af[s & 1][a], accK[a]);
                    accV[a] = mfma32(af[s & 1][a], bf[s & 1][2], accV[a]);
                }
            };
            auto RM = [&](int sr) {
                R(sr); M(sr - 1);
                #pragma unroll
                for (int i = 0; i < 11; i++) { SGB(0x8, 1); SGB(0x100, 1); }
                SGB(0x8, 4);
                sfence();
            };
            const float NEG = -1e30f, CS = 0.25f * 1.44269504088896f;
            const int head = hg * 4 + j;
            const bool par = (i16 & 1) != 0;
            const bool m0 = 4 * g + (i16 & 1) >= i16 + 2, m1 = 4 * g + 2 + (i16 & 1) >= i16 + 2;
            const unsigned sP0 = par ? 0x05040c0cu : 0x0c0c0504u, sP1 = par ? 0x07060c0cu : 0x0c0c0706u, sZ = 0x0c0c0c0cu;
            unsigned sel0 = m0 ? sP0 : sZ, sel1 = m1 ? sP1 : sZ, sel2 = m0 ? sZ : sP0, sel3 = m1 ? sZ : sP1;
            unsigned o_off = WS_OT + ((rt1 * 64 + (head >> 1)) * 64 + (2 * (head & 1) + (g >> 1)) * 16 + i16) * 16 + (g & 1) * 8;
            PIN(sel0); PIN(sel1); PIN(sel2); PIN(sel3); PIN(o_off);
            barx();
            STAMP(16);
            R(0); sfence();
            #pragma unroll
            for (int c = 0; c < 8; c++) {
                RM(2 * c + 1);
                if (c < 7) { bar(); STAMP(17 + c); sfence(); RM(2 * c + 2); }
                else { bar(); M(15); sfence(); }
            }
            STAMP(1);
            f32x2 st[5]; f32x4 s01[5], s23[5];
            #pragma unroll
            for (int a = 0; a < 5; a++) {
                st[a] = *(LAS f32x2*)(sm + L_ST + (a * 16 + i16) * 8);
                s01[a] = *(LAS f32x4*)(sm + L_ST + (a * 16 + 4 * g) * 8);
                s23[a] = *(LAS f32x4*)(sm + L_ST + (a * 16 + 4 * g + 2) * 8);
            }
            const float cv = __shfl(accV[0][0], i16);
            f32x4 cq4, ck4;
            #pragma unroll
            for (int r = 0; r < 4; r++) {
                cq4[r] = __uint_as_float(__builtin_amdgcn_update_dpp(0u, __float_as_uint(accQ[0][r]), 0x150, 0xf, 0xf, true));
                ck4[r] = __uint_as_float(__builtin_amdgcn_update_dpp(0u, __float_as_uint(accK[0][r]), 0x150, 0xf, 0xf, true));
            }
            sfence();
            u32x2 qp[5], kp[5], vp[5];
            #pragma unroll
            for (int a = 0; a < 5; a++) {
                f32x4 t = (accK[a] - st[a][0] * ck4) * st[a][1];
                kp[a][0] = pk_bf16(t[0], t[1]); kp[a][1] = pk_bf16(t[2], t[3]);
                if (a > 0) {
                    t = accQ[a] - st[a][0] * cq4;
                    qp[a][0] = pk_bf16(t[0], t[1]); qp[a][1] = pk_bf16(t[2], t[3]);
                }
            }
            f32x4 s1[5], s2[5];
            const f32x4 z = {};
            #pragma unroll
            for (int a = 1; a < 5; a++) { s1[a] = mfma16(kp[a - 1], qp[a], z); s2[a] = mfma16(kp[a], qp[a], z); }
            #pragma unroll
            for (int a = 0; a < 5; a++) {
                const float t0 = (accV[a][0] - s01[a][0] * cv) * s01[a][1], t1 = (accV[a][1] - s01[a][2] * cv) * s01[a][3];
                const float t2 = (accV[a][2] - s23[a][0] * cv) * s23[a][1], t3 = (accV[a][3] - s23[a][2] * cv) * s23[a][3];
                vp[a][0] = pk_bf16(t0, t1); vp[a][1] = pk_bf16(t2, t3);
            }
            sfence();
            float v0[5], v1[5], mx[5], e0[5], e1[5], sum[5];
            #pragma unroll
            for (int a = 1; a < 5; a++) {
                const f32x4 p1 = s1[a], p2 = s2[a];
                v0[a] = sel(m0, sel(par, p1[1], p1[0]), sel(par, p2[1], p2[0]));
                v1[a] = sel(m1, sel(par, p1[3], p1[2]), sel(par, p2[3], p2[2]));
            }
            v0[1] = sel(m0 && rt1 == 0, NEG, v0[1]); v1[1] = sel(m1 && rt1 == 0, NEG, v1[1]);
            #pragma unroll
            for (int a = 1; a < 5; a++) mx[a] = vmax(v0[a], v1[a]);
            #pragma unroll
            for (int a = 1; a < 5; a++) { auto p = __builtin_amdgcn_permlane16_swap(__float_as_uint(mx[a]), __float_as_uint(mx[a]), false, false); mx[a] = vmax(__uint_as_float(p[0]), __uint_as_float(p[1])); }
            #pragma unroll
            for (int a = 1; a < 5; a++) { auto p = __builtin_amdgcn_permlane32_swap(__float_as_uint(mx[a]), __float_as_uint(mx[a]), false, false); mx[a] = vmax(__uint_as_float(p[0]), __uint_as_float(p[1])); }
            #pragma unroll
            for (int a = 1; a < 5; a++) { const float cs_ = CS * st[a][1]; e0[a] = __builtin_amdgcn_exp2f((v0[a] - mx[a]) * cs_); e1[a] = __builtin_amdgcn_exp2f((v1[a] - mx[a]) * cs_); sum[a] = e0[a] + e1[a]; }
            #pragma unroll
            for (int a = 1; a < 5; a++) { auto p = __builtin_amdgcn_permlane16_swap(__float_as_uint(sum[a]), __float_as_uint(sum[a]), false, false); sum[a] = __uint_as_float(p[0]) + __uint_as_float(p[1]); }
            #pragma unroll
            for (int a = 1; a < 5; a++) { auto p = __builtin_amdgcn_permlane32_swap(__float_as_uint(sum[a]), __float_as_uint(sum[a]), false, false); sum[a] = __uint_as_float(p[0]) + __uint_as_float(p[1]); }
            f32x4 o[5];
            #pragma unroll
            for (int a = 1; a < 5; a++) {
                const float inv = __builtin_amdgcn_rcpf(sum[a]);
                const unsigned pk = pk_bf16(e0[a] * inv, e1[a] * inv);
                u32x4 pf, vf;
                pf[0] = __builtin_amdgcn_perm(pk, 0u, sel0); pf[1] = __builtin_amdgcn_perm(pk, 0u, sel1);
                pf[2] = __builtin_amdgcn_perm(pk, 0u, sel2); pf[3] = __builtin_amdgcn_perm(pk, 0u, sel3);
                vf[0] = vp[a - 1][0]; vf[1] = vp[a - 1][1]; vf[2] = vp[a][0]; vf[3] = vp[a][1];
                o[a] = mfma32(vf, pf, z);
            }
            #pragma unroll
            for (int a = 1; a < 5; a++) {
                u32x2 op; op[0] = pk_bf16(o[a][0], o[a][1]); op[1] = pk_bf16(o[a][2], o[a][3]);
                __builtin_amdgcn_raw_buffer_store_b64(op, rs_ws, o_off, (a - 1) * 16384, 16);
            }
        }
        STAMP(2);
        VMCNT(0); barx();
        STAMP(3);
        u32x4 bo[2][16];
        #pragma unroll
        for (int k = 0; k < 16; k++) {
            LAS char* p = sm + (k >> 1) * 8192 + (k & 1) * 2048 + tr_pair;
            bo[0][k] = tr_frag(p); bo[1][k] = tr_frag(p + 8);
        }
        u32x4 b1[10][3];
        f32x4 cs[3] = {};
        u32x4 ones; ones[0] = ONES; ones[1] = ONES; ones[2] = ONES; ones[3] = ONES;
        auto absorb1 = [&](int G) {
            #pragma unroll
            for (int b = 0; b < 2; b++)
                #pragma unroll
                for (int s = 0; s < 3; s++) b1[2 * G + b][s] = tr_frag(sm + L_RING + (G % 3) * 24576 + s * 8192 + b * 2048 + tr_plain);
        };
        auto colsum1 = [&](int G) {
            #pragma unroll
            for (int b = 0; b < 2; b++)
                #pragma unroll
                for (int s = 0; s < 3; s++) cs[s] = mfma32(b1[2 * G + b][s], ones, cs[s]);
        };
        absorb1(0); absorb1(1);
        colsum1(0); colsum1(1);
        sfence();
        STAMP(4);
        bar();
        u32x4 hres[2];
        unsigned h_off = WS_HT + ((rt * 4 + 2 * kh) * 16 + cb32) * 1024 + l * 16; PIN(h_off);
        auto xch_put = [&](f32x4 (&acc)[2][4]) {
            LAS char* xo = sm + L_XCH + j * 4096 + l * 16;
            #pragma unroll
            for (int ab = 0; ab < 2; ab++)
                #pragma unroll
                for (int m = 0; m < 2; m++) *(LAS f32x4*)(xo + (ab * 2 + m) * 1024) = kh ? acc[ab][m] : acc[ab][2 + m];
        };
        auto xch_get = [&](f32x4 (&acc)[2][4], f32x4 (&res)[2][2]) {
            LAS char* xi = sm + L_XCH + (j ^ 2) * 4096 + l * 16;
            #pragma unroll
            for (int ab = 0; ab < 2; ab++)
                #pragma unroll
                for (int m = 0; m < 2; m++) res[ab][m] = (kh ? acc[ab][2 + m] : acc[ab][m]) + *(LAS f32x4*)(xi + (ab * 2 + m) * 1024);
        };
        {
            f32x4 acc[2][2];
            #pragma unroll
            for (int m = 0; m < 2; m++)
                #pragma unroll
                for (int ab = 0; ab < 2; ab++) {
                    acc[ab][m][0] = bf_lo(xr[m][2 * ab]); acc[ab][m][1] = bf_hi(xr[m][2 * ab]);
                    acc[ab][m][2] = bf_lo(xr[m][2 * ab + 1]); acc[ab][m][3] = bf_hi(xr[m][2 * ab + 1]);
                }
            u32x4 a2[2][2][2];
            auto R2 = [&](int k2) {
                #pragma unroll
                for (int k = 0; k < 2; k++)
                    #pragma unroll
                    for (int m = 0; m < 2; m++) a2[k2 & 1][k][m] = lds_r128(sm + (2 * kh + m) * 16384 + (2 * k2 + k) * 1024 + l * 16);
            };
            sfence();
            barx();
            STAMP(5);
            R2(0); sfence();
            #pragma unroll
            for (int k2 = 0; k2 < 8; k2++) {
                if (k2 == 3) barx();
                if (k2 < 7) R2(k2 + 1);
                #pragma unroll
                for (int k = 0; k < 2; k++)
                    #pragma unroll
                    for (int m = 0; m < 2; m++) {
                        acc[0][m] = mfma32(bo[0][2 * k2 + k], a2[k2 & 1][k][m], acc[0][m]);
                        acc[1][m] = mfma32(bo[1][2 * k2 + k], a2[k2 & 1][k][m], acc[1][m]);
                    }
                #pragma unroll
                for (int i = 0; i < 4; i++) { SGB(0x8, 1); SGB(0x100, 1); }
                SGB(0x8, 4);
                sfence();
            }
            STAMP(6);
            #pragma unroll
            for (int m = 0; m < 2; m++) {
                hres[m][0] = pk_bf16(acc[0][m][0], acc[0][m][1]); hres[m][1] = pk_bf16(acc[0][m][2], acc[0][m][3]);
                hres[m][2] = pk_bf16(acc[1][m][0], acc[1][m][1]); hres[m][3] = pk_bf16(acc[1][m][2], acc[1][m][3]);
                __builtin_amdgcn_raw_buffer_store_b128(hres[m], rs_ws, h_off, m * 16384, 16);
                *(LAS u32x4*)(sm + L_XCH + j * 4096 + m * 1024 + l * 16) = hres[m];
            }
        }
        STAMP(7);
        VMCNT(0); bar();
        STAMP(8);
        absorb1(2); absorb1(3); absorb1(4);
        bar();
        colsum1(2); colsum1(3); colsum1(4);
        sfence();
        STAMP(9);
        barx();
        {
            f32x4 acc[3][4] = {};
            u32x4 a3[4], br[2][3];
            float st[4]; unsigned f_off[3];
            auto a3_off = [&](int pos) {
                const int p = pos >> 1;
                return (unsigned)((((p == 0 ? 0 : p < 4 ? p + 4 : p - 3) + urot) & 7) * 2048 + (pos & 1) * 1024);
            };
            barx();
            STAMP(10);
            #pragma unroll
            for (int m = 0; m < 4; m++) a3[m] = lds_r128(sm + m * 16384 + a3_off(0) + l * 16);
            sfence();
            #pragma unroll
            for (int pos = 0; pos < 16; pos++) {
                if ((pos & 3) == 3 && pos < 15) bar();
                const bool res = pos >= 2 && pos < 8, nres = pos >= 1 && pos < 7;
                if (pos == 12) {
                    #pragma unroll
                    for (int m = 0; m < 4; m++) st[m] = *(LAS float*)(sm + L_S3 + (m * 16 + i16) * 8);
                    #pragma unroll
                    for (int s = 0; s < 3; s++) {
                        const int T = mem * 12 + s * 4 + j;
                        f_off[s] = T < 86 ? WS_FT + ((rt * 4 * 43 + (T >> 1)) * 64 + (2 * (T & 1) + (g >> 1)) * 16 + i16) * 16 + (g & 1) * 8 : OOB;
                        PIN(f_off[s]);
                    }
                }
                #pragma unroll
                for (int m = 0; m < 4; m++) {
                    if (nres && m < 3) br[(pos + 1) & 1][m] = tr_frag(sm + L_RING + ((((pos + 1) >> 1) + 4) % 3) * 24576 + m * 8192 + ((pos + 1) & 1) * 2048 + tr_plain);
                    #pragma unroll
                    for (int s = 0; s < 3; s++) {
                        const u32x4 b = res ? br[pos & 1][s] : b1[pos < 2 ? pos : pos < 8 ? 0 : pos - 6][s];
                        acc[s][m] = mfma32(b, a3[m], acc[s][m]);
                    }
                    if (res && m < 3) cs[m] = mfma32(br[pos & 1][m], ones, cs[m]);
                    SGB(0x8, 1); SGB(0x100, 1); SGB(0x8, 1); SGB(0x100, 1); SGB(0x8, 2);
                    sfence();
                    if (pos < 15) a3[m] = lds_r128(sm + m * 16384 + a3_off(pos + 1) + l * 16);
                    sfence();
                }
            }
            STAMP(11);
            barx();
            #pragma unroll
            for (int m = 0; m < 4; m++)
                #pragma unroll
                for (int s = 0; s < 3; s++) {
                    const f32x4 t = acc[s][m] - st[m] * cs[s];
                    u32x2 o; o[0] = relu_pk(pk_bf16(t[0], t[1])); o[1] = relu_pk(pk_bf16(t[2], t[3]));
                    __builtin_amdgcn_raw_buffer_store_b64(o, rs_ws, f_off[s], m * (43 * 1024), 16);
                }
        }
        STAMP(12);
        VMCNT(0); barx();
        STAMP(13);
        {
            u32x4 bw[NA][2];
            float rowsc[2];
            auto absorb2 = [&](int G) {
                #pragma unroll
                for (int u = 0; u < 3; u++) {
                    LAS char* p = sm + L_RING + ((G + 2) % 3) * 24576 + u * 8192 + kh * 2048 + tr_pair;
                    bw[3 * G + u][0] = tr_frag(p); bw[3 * G + u][1] = tr_frag(p + 8);
                }
            };
            absorb2(0); absorb2(1); absorb2(2);
            #pragma unroll
            for (int m = 0; m < 2; m++) { hres[m] = lds_r128(sm + L_XCH + j * 4096 + m * 1024 + l * 16); PIN(hres[m]); rowsc[m] = *(LAS float*)(sm + L_S3 + ((2 * kh + m) * 16 + i16) * 8 + 4); PIN(rowsc[m]); }
            bar();
            barx();
            absorb2(3); absorb2(4); absorb2(5);
            STAMP(14);
            f32x4 acc[2][4] = {};
            u32x4 a4[2][4], br[2][2];
            auto R4 = [&](int u) {
                #pragma unroll
                for (int m = 0; m < 4; m++) a4[u & 1][m] = lds_r128(sm + ((u >> 1) & 3) * 16384 + m * 4096 + kh * 2048 + (u & 1) * 1024 + l * 16);
                if (u >= NA) {
                    LAS char* p = sm + L_RING + ((u / 3 + 2) % 3) * 24576 + (u % 3) * 8192 + kh * 2048 + tr_pair;
                    br[u & 1][0] = tr_frag(p); br[u & 1][1] = tr_frag(p + 8);
                }
            };
            bar();
            STAMP(15);
            R4(0); sfence();
            #pragma unroll
            for (int u = 0; u < 22; u++) {
                if ((u & 1) && u < 21) bar();
                if (u < 21) R4(u + 1);
                #pragma unroll
                for (int ab = 0; ab < 2; ab++) {
                    const u32x4 b = u < NA ? bw[u < NA ? u : 0][ab] : br[u & 1][ab];
                    #pragma unroll
                    for (int m = 0; m < 4; m++) acc[ab][m] = mfma32(b, a4[u & 1][m], acc[ab][m]);
                }
                #pragma unroll
                for (int i = 0; i < 8; i++) { SGB(0x8, 1); SGB(0x100, 1); }
                sfence();
            }
            STAMP(24);
            xch_put(acc);
            bar();
            f32x4 r[2][2];
            xch_get(acc, r);
            #pragma unroll
            for (int m = 0; m < 2; m++) {
                u32x4 o;
                o[0] = pk_bf16(bf_lo(hres[m][0]) + rowsc[m] * r[0][m][0], bf_hi(hres[m][0]) + rowsc[m] * r[0][m][1]);
                o[1] = pk_bf16(bf_lo(hres[m][1]) + rowsc[m] * r[0][m][2], bf_hi(hres[m][1]) + rowsc[m] * r[0][m][3]);
                o[2] = pk_bf16(bf_lo(hres[m][2]) + rowsc[m] * r[1][m][0], bf_hi(hres[m][2]) + rowsc[m] * r[1][m][1]);
                o[3] = pk_bf16(bf_lo(hres[m][3]) + rowsc[m] * r[1][m][2], bf_hi(hres[m][3]) + rowsc[m] * r[1][m][3]);
                *(u32x4*)(out + ((rt * 4 + 2 * kh + m) * 16 + i16) * 512 + cb32 * 32 + 8 * g) = o;
            }
        }
        STAMP(25);
    }
}

extern "C" void kernel_launch(void* const* d_in, const int* in_sizes, int n_in, void* d_out, int out_size, void* d_ws, size_t ws_size, hipStream_t stream) {
    (void)in_sizes; (void)n_in; (void)out_size; (void)ws_size;
    (void)hipFuncSetAttribute((const void*)fused_block_kernel, hipFuncAttributeMaxDynamicSharedMemorySize, LDS_BYTES);
    fused_block_kernel<<<256, 512, LDS_BYTES, stream>>>((const u16*)d_in[0], (const u16*)d_in[1], (const u16*)d_in[2], (const u16*)d_in[4], (const u16*)d_in[5], (u16*)d_out, (char*)d_ws);
}
```

```cpp
#define PF_CODE
#define PF_TLB
#include <hip/hip_runtime.h>
#include <hip/hip_bf16.h>
#include <stdint.h>

typedef unsigned short u16;
typedef unsigned long long u64;
typedef __attribute__((ext_vector_type(8))) short s16x8;
typedef __attribute__((ext_vector_type(4))) short s16x4;
typedef __attribute__((ext_vector_type(4))) float f32x4;
typedef __attribute__((ext_vector_type(2))) float f32x2;
typedef __attribute__((ext_vector_type(4))) unsigned u32x4;
typedef __attribute__((ext_vector_type(2))) unsigned u32x2;
typedef __attribute__((ext_vector_type(2))) __bf16 bf16x2;
#define GAS __attribute__((address_space(1)))
#define LAS __attribute__((address_space(3)))
typedef GAS unsigned gu32;
#define RLX_AGENT __ATOMIC_RELAXED, __HIP_MEMORY_SCOPE_AGENT
#define DEV __device__ __forceinline__

#define SLP 1
#define ABL 0
namespace {
constexpr int S_ = 2048, D_ = 512, DFF = 1376;
constexpr unsigned WS_EPOCH = 0;
constexpr unsigned WS_SFLAG = 8192;
constexpr unsigned WS_ST1 = 131072;
constexpr unsigned WS_ST2 = 262144;
constexpr unsigned WS_DIAG  = 65536;
constexpr unsigned WS_OT  = 1u << 20;
constexpr unsigned WS_HT  = 3u << 20;
constexpr unsigned WS_FT  = 5u << 20;
constexpr unsigned WS_USED = 11u << 20;
constexpr unsigned L_SLOT = 34816;
constexpr unsigned L_RING = 69632;
constexpr unsigned L_XCH = 143360;
constexpr unsigned L_CTL = 159744;
constexpr unsigned L_ST = L_CTL + 256;
constexpr unsigned L_S3 = L_CTL + 1024;
constexpr unsigned L_DG = L_CTL + 2048;
constexpr unsigned LDS_BYTES = 163840;
constexpr unsigned SPIN_CAP = 1u << 15;
constexpr int NA = 18;

DEV float bf_lo(unsigned v) { return __uint_as_float(v << 16); }
DEV float bf_hi(unsigned v) { return __uint_as_float(v & 0xffff0000u); }
DEV unsigned pk_bf16(float a, float b) { bf16x2 r; r[0] = (__bf16)a; r[1] = (__bf16)b; return __builtin_bit_cast(unsigned, r); }
DEV float rbf(float a) { return (float)((__bf16)a); }
DEV float dot2(unsigned a, unsigned b, float c) {
    return __builtin_amdgcn_fdot2_f32_bf16(__builtin_bit_cast(bf16x2, a), __builtin_bit_cast(bf16x2, b), c, false);
}
DEV f32x4 mfma32(u32x4 a, u32x4 b, f32x4 c) {
    return __builtin_amdgcn_mfma_f32_16x16x32_bf16(__builtin_bit_cast(s16x8, a), __builtin_bit_cast(s16x8, b), c, 0, 0, 0);
}
DEV f32x4 mfma16(u32x2 a, u32x2 b, f32x4 c) {
    return __builtin_amdgcn_mfma_f32_16x16x16bf16_1k(__builtin_bit_cast(s16x4, a), __builtin_bit_cast(s16x4, b), c, 0, 0, 0);
}
DEV u32x2 tr_read(LAS char* p) { return __builtin_bit_cast(u32x2, __builtin_amdgcn_ds_read_tr16_b64_v4i16((LAS s16x4*)p)); }
DEV u32x4 tr_frag(LAS char* p) { u32x2 lo = tr_read(p), hi = tr_read(p + 512); u32x4 v; v[0] = lo[0]; v[1] = lo[1]; v[2] = hi[0]; v[3] = hi[1]; return v; }
DEV u32x4 lds_r128(LAS char* p) { return *(LAS u32x4*)p; }
template <int CTRL> DEV float dpp_add(float v) {
    return v + __uint_as_float(__builtin_amdgcn_update_dpp(0, __float_as_uint(v), CTRL, 0xf, 0xf, true));
}
DEV float xsum16_32(float v) {
    auto a = __builtin_amdgcn_permlane16_swap(__float_as_uint(v), __float_as_uint(v), false, false);
    v = __uint_as_float(a[0]) + __uint_as_float(a[1]);
    auto b = __builtin_amdgcn_permlane32_swap(__float_as_uint(v), __float_as_uint(v), false, false);
    return __uint_as_float(b[0]) + __uint_as_float(b[1]);
}
DEV float xmax16_32(float v) {
    auto a = __builtin_amdgcn_permlane16_swap(__float_as_uint(v), __float_as_uint(v), false, false);
    v = fmaxf(__uint_as_float(a[0]), __uint_as_float(a[1]));
    auto b = __builtin_amdgcn_permlane32_swap(__float_as_uint(v), __float_as_uint(v), false, false);
    return fmaxf(__uint_as_float(b[0]), __uint_as_float(b[1]));
}
DEV f32x2 ln_stats(float s, float q) {
    float m = s * (1.0f / 512.0f);
    float var = fmaxf(q * (1.0f / 512.0f) - m * m, 0.0f);
    float sd = rbf(__builtin_amdgcn_sqrtf(rbf(rbf(var) + 1e-5f)));
    f32x2 r; r[0] = rbf(m); r[1] = __builtin_amdgcn_rcpf(sd); return r;
}
DEV unsigned ln_pack(float s, float q) {
    float m = s * (1.0f / 512.0f);
    float var = fmaxf(q * (1.0f / 512.0f) - m * m, 0.0f);
    return pk_bf16(m, __builtin_amdgcn_sqrtf(rbf(rbf(var) + 1e-5f)));
}
typedef __attribute__((ext_vector_type(2))) short s16x2;
DEV unsigned relu_pk(unsigned v) { const s16x2 z = {0, 0}; return __builtin_bit_cast(unsigned, __builtin_elementwise_max(__builtin_bit_cast(s16x2, v), z)); }
DEV float sel(bool c, float a, float b) { return c ? a : b; }
DEV float vmax(float a, float b) { float r; asm("v_max_f32 %0, %1, %2" : "=v"(r) : "v"(a), "v"(b)); return r; }
DEV void fence() { asm volatile("" ::: "memory"); }
DEV void sfence() { asm volatile("" ::: "memory"); __builtin_amdgcn_sched_barrier(0); }
#define VMCNT(n) asm volatile("s_waitcnt vmcnt(" #n ")" ::: "memory")
#define SGB(mask, n) __builtin_amdgcn_sched_group_barrier(mask, n, 0)
#define PIN(x) asm volatile("" : "+v"(x))
#define RFL(x) ((unsigned)__builtin_amdgcn_readfirstlane(x))
#define DMA(rs, ldsoff, voff, soff, imm, aux) __builtin_amdgcn_raw_ptr_buffer_load_lds(rs, (LAS void*)(sm + (ldsoff)), 16, voff, soff, imm, aux)
#define DMA4(rs, ldsoff, vo, soff, aux) do { const unsigned lo_ = (ldsoff), so_ = (soff); DMA(rs, lo_, vo[0], so_, 0, aux); DMA(rs, lo_, vo[1], so_, 1024, aux); \
    DMA(rs, lo_, vo[2], so_, 2048, aux); DMA(rs, lo_, vo[3], so_, 3072, aux); } while (0)
DEV void bar() { asm volatile("s_waitcnt lgkmcnt(0)" ::: "memory"); __builtin_amdgcn_s_barrier(); asm volatile("" ::: "memory"); }
DEV void barx() { asm volatile("" ::: "memory"); __builtin_amdgcn_s_barrier(); asm volatile("" ::: "memory"); }
#define STAMP(i) do {} while (0)
}

__global__ __launch_bounds__(512, 2) void fused_block_kernel(const u16* __restrict__ x, const u16* __restrict__ wqkv, const u16* __restrict__ wo,
                                                            const u16* __restrict__ w1, const u16* __restrict__ w2, u16* __restrict__ out, char* ws) {
    extern __shared__ __attribute__((aligned(16))) char smem_[];
    LAS char* sm = (LAS char*)smem_;
    u64 kbase; asm volatile("s_getpc_b64 %0" : "=s"(kbase));
    const int bid = blockIdx.x, tid = threadIdx.x;
    const int w = __builtin_amdgcn_readfirstlane(tid >> 6), l = tid & 63;
    const int g = l >> 4, i16 = l & 15;
    const int xcd = bid & 7, idx = bid >> 3;
    const int rt = xcd * 4 + (idx >> 3), mem = idx & 7;
    const int rt1 = (xcd >> 1) * 8 + (idx >> 2), hg = (xcd & 1) * 4 + (idx & 3);
    const int urot = (idx >> 3) * 2;
    const int crot = ((rt1 & 7) + 2 * (hg & 3)) & 7;
    const int r0 = rt1 * 64;
    const int row8 = l >> 3, ch = l & 7;
    const auto rs_x = __builtin_amdgcn_make_buffer_rsrc((void*)x, 0, S_ * D_ * 2, 0x00020000);
    const auto rs_ws = __builtin_amdgcn_make_buffer_rsrc((void*)ws, 0, WS_USED, 0x00020000);
    const unsigned OOB = 0x80000000u;
    const unsigned ONES = 0x3f803f80u;
    STAMP(0);

    if (w == 4 || w == 5) {
        const int lw = w - 4;
        const auto rs_wqkv = __builtin_amdgcn_make_buffer_rsrc((void*)wqkv, 0, D_ * 3 * D_ * 2, 0x00020000);
        unsigned a_vo[5], q_vo[4];
        #pragma unroll
        for (int e = 0; e < 5; e++) {
            int ar = r0 - 16 + (lw * 5 + e) * 8 + row8; ar = ar < 0 ? 0 : ar;
            a_vo[e] = (ar * 512 + ((ch ^ row8) * 8)) * 2 + 4096 - (e ? (e - 1) * 1024 : 0);
        }
        const auto rs_xm = __builtin_amdgcn_make_buffer_rsrc((void*)((const char*)x - 4096), 0, S_ * D_ * 2 + 4096, 0x00020000);
        const unsigned swz = (ch ^ ((((row8 >> 1) & 1) | (lw << 1)) << 1)) * 16;
        #pragma unroll
        for (int i = 0; i < 4; i++) q_vo[i] = (lw * 8 + i * 16 + row8) * 3072 + hg * 128 + swz - i * 1024;
        auto P1 = [&](int c) {
            const int cc = (c + crot) & 7;
            const unsigned sl = (c & 3) * L_SLOT;
            unsigned so = cc * 128; asm volatile("" : "+s"(so));
            DMA4(rs_xm, sl + lw * 5120 + 1024, (a_vo + 1), so, 0);
            if (lw == 1 || l >= 8) DMA(rs_xm, sl + lw * 5120, a_vo[0], so, 0, 0);
            fence();
            #pragma unroll
            for (int s = 0; s < 3; s++) DMA4(rs_wqkv, sl + 10240 + s * 8192 + lw * 4096, q_vo, cc * (64 * 3072) + s * 1024, 0);
        };
        P1(0); P1(1);
        VMCNT(17);
        barx();
        #pragma nounroll
        for (int r = 0; r < 6; r++) { P1(r + 2); VMCNT(17); barx(); }
        const auto rs_wo = __builtin_amdgcn_make_buffer_rsrc((void*)wo, 0, D_ * D_ * 2, 0x00020000);
        const auto rs_w1 = __builtin_amdgcn_make_buffer_rsrc((void*)w1, 0, D_ * DFF * 2, 0x00020000);
        const auto rs_w2 = __builtin_amdgcn_make_buffer_rsrc((void*)w2, 0, D_ * DFF * 2, 0x00020000);
        unsigned wo_vo[4], w1_vo[4], w2_vo[4], lin_vo[4], f_vo[4];
        #pragma unroll
        for (int i = 0; i < 4; i++) {
            wo_vo[i] = (lw * 8 + i * 16 + row8) * 1024 + mem * 128 + swz - i * 1024;
            w1_vo[i] = (lw * 8 + i * 16 + row8) * (DFF * 2) + mem * 384 + swz - i * 1024;
            w2_vo[i] = (lw * 8 + (i & 1) * 16 + (i >> 1) * 704 + row8) * 1024 + mem * 128 + swz - i * 1024;
            lin_vo[i] = l * 16;
            f_vo[i] = l * 16 + (i >> 1) * (20 * 1024);
        }
        auto WO = [&](int G) {
            #pragma unroll
            for (int i = 0; i < 4; i++) { const unsigned u = G * 4 + ((i + (urot >> 1)) & 3); DMA4(rs_wo, u * 8192 + lw * 4096, wo_vo, u * 65536, 0); }
        };
        auto W1 = [&](int G) {
            const unsigned u = (G + urot) & 7;
            #pragma unroll
            for (int s = 0; s < 3; s++) DMA4(rs_w1, L_RING + (G % 3) * 24576 + s * 8192 + lw * 4096, w1_vo, u * (64 * DFF * 2) + s * 128, 0);
        };
        auto W2 = [&](int G, int n) {
            #pragma unroll
            for (int u = 0; u < 3; u++) if (u < n) DMA4(rs_w2, L_RING + ((G + 2) % 3) * 24576 + u * 8192 + lw * 4096, w2_vo, (3 * G + u) * 32768, 0);
        };
        WO(0); VMCNT(16); barx();
        barx();
        WO(1); W1(0); W1(1); W1(2); VMCNT(12); barx();
        STAMP(1);
        barx();
        STAMP(2);
        {
            const unsigned src = WS_OT + (rt * 4 + lw) * 16384;
            #pragma unroll
            for (int hf = 0; hf < 2; hf++)
                #pragma unroll
                for (int i = 0; i < 4; i++) {
                    const unsigned o = (i >> 1) * 32768 + ((i & 1) * 4 + hf * 8) * 1024;
                    DMA4(rs_ws, lw * 16384 + o, lin_vo, src + o, 16);
                }
        }
        VMCNT(16); barx();
        STAMP(3);
        W1(3); VMCNT(12); barx();
        W1(4); VMCNT(0); barx();
        barx();
        W1(5); W1(6); W1(7);
        STAMP(4);
        barx();
        STAMP(5);
        {
            const unsigned src = WS_HT + (rt * 4 + lw) * 16384;
            #pragma unroll
            for (int p = 0; p < 8; p++) {
                const unsigned u = ((p == 0 ? 0 : p < 4 ? p + 4 : p - 3) + urot) & 7;
                #pragma unroll
                for (int m = 0; m < 2; m++) {
                    const unsigned o = m * 32768 + u * 2048;
                    DMA(rs_ws, lw * 16384 + o, lin_vo[0], src + o, 0, 16); DMA(rs_ws, lw * 16384 + o, lin_vo[0], src + o, 1024, 16);
                }
            }
        }
        VMCNT(24); barx();
        STAMP(6);
        VMCNT(16); barx();
        W2(0, 3); VMCNT(20); barx();
        W2(1, 3); W2(2, 3); VMCNT(36); barx();
        barx();
        VMCNT(0); barx();
        barx();
        W2(3, 3); W2(4, 3); W2(5, 3); VMCNT(0);
        STAMP(7);
        barx();
        STAMP(8);
        const unsigned fsrc = WS_FT + (rt * 4 + lw) * (43 * 1024);
        auto FG = [&](unsigned F) {
            #pragma unroll
            for (int m = 0; m < 2; m++) DMA4(rs_ws, (F & 3) * 16384 + (2 * m + lw) * 4096, f_vo, fsrc + m * (86 * 1024) + F * 2048, 16);
        };
        FG(0); FG(1); FG(2); FG(3);
        VMCNT(24); barx();
        STAMP(9);
        W2(6, 3); W2(7, 1);
        VMCNT(32); barx();
        FG(4); VMCNT(32); barx();
        FG(5); VMCNT(32); barx();
        #pragma nounroll
        for (unsigned F = 6; F < 10; F++) { FG(F); VMCNT(16); barx(); }
        f_vo[3] = OOB;
        FG(10); VMCNT(16); barx();
        VMCNT(8); barx();
        VMCNT(0); barx();
        STAMP(10);
        barx();
    } else if (w >= 6) {
        const int hw = w - 6;
        unsigned pfv = 0;
        if (hw == 1 && idx < 27) {
            u64 pc; asm volatile("s_getpc_b64 %0" : "=s"(pc));
            pc = kbase;
            pfv = *(const GAS unsigned*)(pc + idx * 1024 + l * 16);
        }
        if (hw == 0) {
            const char* p;
            if (l < 8) p = (const char*)wo + l * 65536;
            else if (l < 30) p = (const char*)w1 + (l - 8) * 65536;
            else if (l < 52) p = (const char*)w2 + (l - 30) * 65536;
            else if (l == 52) p = (const char*)out + rt * 65536 + mem * 128;
            else if (l == 53) p = ws + WS_OT + (rt1 * 64 + hg * 2) * 1024;
            else if (l == 54) p = ws + WS_HT + (rt * 64 + mem * 2) * 1024;
            else if (l < 59) p = ws + WS_FT + ((rt * 4 + (l - 55)) * 43 + mem * 5) * 1024;
            else p = ws + WS_ST2 + (rt * 8 + mem) * 1024;
            pfv = __hip_atomic_load((const gu32*)p, RLX_AGENT);
        }
        u32x4 xv[8];
        {
            int ar = r0 - 16 + hg * 10 + hw * 5 + row8; ar = ar < 0 ? 0 : ar;
            const unsigned vo = l < 40 ? (unsigned)(ar * 1024 + ch * 16) : OOB;
            #pragma unroll
            for (int i = 0; i < 8; i++) xv[i] = __builtin_amdgcn_raw_buffer_load_b128(rs_x, vo + i * 128, 0, 0);
        }
        unsigned tag = __hip_atomic_load((gu32*)(ws + WS_EPOCH + (l == 0 ? rt1 : rt) * 256), RLX_AGENT) + 1u;
        if (hw == 1 && l < 32) { u32x4 o; o[0] = ONES; o[1] = ONES; o[2] = ONES; o[3] = ONES; *(LAS u32x4*)(sm + (l >> 3) * L_SLOT + (l & 7) * 16) = o; }
        sfence();
        bar();
        float ss[2] = {}, qq[2] = {};
        auto st1 = [&](int i0, int i1) {
            #pragma unroll
            for (int i = i0; i < i1; i++)
                #pragma unroll
                for (int j = 0; j < 4; j++) { ss[j & 1] = dot2(xv[i][j], ONES, ss[j & 1]); qq[j & 1] = dot2(xv[i][j], xv[i][j], qq[j & 1]); }
            PIN(ss[0]); PIN(ss[1]); PIN(qq[0]); PIN(qq[1]);
            sfence();
        };
        st1(0, 3); barx();
        st1(3, 6); barx();
        st1(6, 8); barx();
        asm volatile("" :: "v"(pfv));
        const unsigned tag1 = (unsigned)__builtin_amdgcn_readlane(tag, 0), tagr = (unsigned)__builtin_amdgcn_readlane(tag, 1);
        {
            float s = ss[0] + ss[1], q = qq[0] + qq[1];
            s = dpp_add<0xB1>(s); q = dpp_add<0xB1>(q); s = dpp_add<0x4E>(s); q = dpp_add<0x4E>(q); s = dpp_add<0x141>(s); q = dpp_add<0x141>(q);
            u32x2 gr; gr[0] = ln_pack(s, q); gr[1] = tag1;
            __builtin_amdgcn_raw_buffer_store_b64(gr, rs_ws, (ch == 0 && l < 40) ? WS_ST1 + (rt1 * 80 + hg * 10 + hw * 5 + row8) * 8 : OOB, 0, 16);
        }
        sfence();
        barx(); barx();
        const unsigned g1_off = l < 40 ? WS_ST1 + (rt1 * 80 + hw * 40 + l) * 8 : OOB;
        u32x2 gr = __builtin_amdgcn_raw_buffer_load_b64(rs_ws, g1_off, 0, 16);
        sfence();
        barx();
        #pragma nounroll
        for (unsigned s = 0; s < SPIN_CAP; s++) {
            if (__all(l >= 40 || gr[1] == tag1)) break;
            __builtin_amdgcn_s_sleep(1);
            gr = __builtin_amdgcn_raw_buffer_load_b64(rs_ws, g1_off, 0, 16);
        }
        if (l < 40) { f32x2 v; v[0] = bf_lo(gr[0]); v[1] = __builtin_amdgcn_rcpf(bf_hi(gr[0])); *(LAS f32x2*)(sm + L_ST + (hw * 40 + l) * 8) = v; }
        STAMP(1);
        bar();
        barx();
        auto signal = [&](int seam, int grp, int slot, unsigned tg) {
            if (hw == 0 && l == 0) __hip_atomic_store((gu32*)(ws + WS_SFLAG + (seam * 32 + grp) * 256 + slot * 4), tg, RLX_AGENT);
        };
        auto poll = [&](int seam) {
            if (hw == 0) {
                #pragma nounroll
                for (unsigned s = 0; s < SPIN_CAP; s++) {
                    const unsigned v = __hip_atomic_load((gu32*)(ws + WS_SFLAG + (seam * 32 + rt) * 256 + (l & 7) * 4), RLX_AGENT);
                    if (__all(v == tagr)) break;
                    __builtin_amdgcn_s_sleep(1);
                }
            }
        };
        barx();
        signal(0, rt1, hg, tag1);
        poll(0);
        STAMP(2);
        barx(); barx(); barx();
        barx();
        signal(1, rt, mem, tagr);
        {
            u32x4 v[2][2];
            #pragma unroll
            for (int m = 0; m < 2; m++)
                #pragma unroll
                for (int n = 0; n < 2; n++) v[m][n] = lds_r128(sm + L_XCH + (hw * 2 + n) * 4096 + m * 1024 + l * 16);
            bar();
            float s2[2] = {}, q2[2] = {};
            #pragma unroll
            for (int n = 0; n < 2; n++)
                #pragma unroll
                for (int j = 0; j < 4; j++)
                    #pragma unroll
                    for (int m = 0; m < 2; m++) { s2[m] = dot2(v[m][n][j], ONES, s2[m]); q2[m] = dot2(v[m][n][j], v[m][n][j], q2[m]); }
            const float sa = xsum16_32(s2[0]), qa = xsum16_32(q2[0]), sb = xsum16_32(s2[1]), qb = xsum16_32(q2[1]);
            u32x4 o; o[0] = __float_as_uint(g ? sb : sa); o[1] = tagr; o[2] = __float_as_uint(g ? qb : qa); o[3] = tagr;
            __builtin_amdgcn_raw_buffer_store_b128(o, rs_ws, l < 32 ? WS_ST2 + ((rt * 8 + mem) * 64 + hw * 32 + l) * 16 : OOB, 0, 16);
            sfence();
        }
        poll(1);
        STAMP(3);
        barx();
        if (hw == 0 && mem == 0 && l == 0) __hip_atomic_store((gu32*)(ws + WS_EPOCH + rt * 256), tagr, RLX_AGENT);
        barx(); barx();
        if (hw == 1) {
            #pragma nounroll
            for (unsigned s = 0; s < SPIN_CAP; s++) {
                u32x4 p[8];
                #pragma unroll
                for (int i = 0; i < 8; i++) p[i] = __builtin_amdgcn_raw_buffer_load_b128(rs_ws, WS_ST2 + (rt * 512 + l) * 16 + i * 1024, 0, 16);
                bool ok = true; float sx = 0.f, qx = 0.f;
                #pragma unroll
                for (int i = 0; i < 8; i++) { ok = ok && p[i][1] == tagr && p[i][3] == tagr; sx += __uint_as_float(p[i][0]); qx += __uint_as_float(p[i][2]); }
                *(LAS f32x2*)(sm + L_S3 + l * 8) = ln_stats(sx, qx);
                if (__all(ok)) break;
                __builtin_amdgcn_s_sleep(1);
            }
        }
        STAMP(4);
        bar(); barx(); barx();
        barx();
        signal(2, rt, mem, tagr);
        barx();
        poll(2);
        STAMP(5);
        #pragma nounroll
        for (int i = 24; i <= 36; i++) barx();
    } else {
        const int j = w;
        const int n2 = j & 1, kh = j >> 1;
        const int q4 = i16 >> 2, p4 = l & 3;
        const unsigned sw_r = ((q4 >> 1) | ((g & 1) << 1)) << 1;
        const unsigned tr_row = ((g & 1) * 4 + (g >> 1)) * 1024 + q4 * 128;
        const unsigned tr_plain = tr_row + (((2 * j + (p4 >> 1)) ^ sw_r) * 16) + (p4 & 1) * 8;
        const unsigned tr_pair = tr_row + (((4 * n2 + p4) ^ sw_r) * 16);
        const int cb32 = mem * 2 + n2;
        u32x4 xr[2];
        #pragma unroll
        for (int m = 0; m < 2; m++) xr[m] = __builtin_amdgcn_raw_buffer_load_b128(rs_x, (((rt * 4 + 2 * kh + m) * 16 + i16) * 512 + cb32 * 32 + 8 * g) * 2, 0, 0);
        {
            const unsigned a_rd0 = i16 * 128 + ((g ^ (i16 & 7)) * 16);
            const unsigned a_rd1 = a_rd0 ^ 64;
            f32x4 accQ[5] = {}, accK[5] = {}, accV[5] = {};
            u32x4 af[2][5], bf[2][3];
            auto R = [&](int s) {
                LAS char* sl = sm + ((s >> 1) & 3) * L_SLOT;
                #pragma unroll
                for (int t = 0; t < 3; t++) bf[s & 1][t] = tr_frag(sl + 10240 + tr_plain + t * 8192 + (s & 1) * 2048);
                #pragma unroll
                for (int t = 0; t < 5; t++) af[s & 1][t] = lds_r128(sl + ((s & 1) ? a_rd1 : a_rd0) + t * 2048);
            };
            auto M = [&](int s) {
                #pragma unroll
                for (int a = 0; a < 5; a++) {
                    accQ[a] = mfma32(bf[s & 1][0], af[s & 1][a], accQ[a]);
                    accK[a] = mfma32(bf[s & 1][1], af[s & 1][a], accK[a]);
                    accV[a] = mfma32(af[s & 1][a], bf[s & 1][2], accV[a]);
                }
            };
            auto RM = [&](int sr) {
                R(sr); M(sr - 1);
                #pragma unroll
                for (int i = 0; i < 11; i++) { SGB(0x8, 1); SGB(0x100, 1); }
                SGB(0x8, 4);
                sfence();
            };
            const float NEG = -1e30f, CS = 0.25f * 1.44269504088896f;
            const int head = hg * 4 + j;
            const bool par = (i16 & 1) != 0;
            const bool m0 = 4 * g + (i16 & 1) >= i16 + 2, m1 = 4 * g + 2 + (i16 & 1) >= i16 + 2;
            const unsigned sP0 = par ? 0x05040c0cu : 0x0c0c0504u, sP1 = par ? 0x07060c0cu : 0x0c0c0706u, sZ = 0x0c0c0c0cu;
            unsigned sel0 = m0 ? sP0 : sZ, sel1 = m1 ? sP1 : sZ, sel2 = m0 ? sZ : sP0, sel3 = m1 ? sZ : sP1;
            unsigned o_off = WS_OT + ((rt1 * 64 + (head >> 1)) * 64 + (2 * (head & 1) + (g >> 1)) * 16 + i16) * 16 + (g & 1) * 8;
            PIN(sel0); PIN(sel1); PIN(sel2); PIN(sel3); PIN(o_off);
            barx();
            STAMP(16);
            R(0); sfence();
            #pragma unroll
            for (int c = 0; c < 8; c++) {
                RM(2 * c + 1);
                if (c < 7) { bar(); STAMP(17 + c); sfence(); RM(2 * c + 2); }
                else { bar(); M(15); sfence(); }
            }
            STAMP(1);
            f32x2 st[5]; f32x4 s01[5], s23[5];
            #pragma unroll
            for (int a = 0; a < 5; a++) {
                st[a] = *(LAS f32x2*)(sm + L_ST + (a * 16 + i16) * 8);
                s01[a] = *(LAS f32x4*)(sm + L_ST + (a * 16 + 4 * g) * 8);
                s23[a] = *(LAS f32x4*)(sm + L_ST + (a * 16 + 4 * g + 2) * 8);
            }
            const float cv = __shfl(accV[0][0], i16);
            f32x4 cq4, ck4;
            #pragma unroll
            for (int r = 0; r < 4; r++) {
                cq4[r] = __uint_as_float(__builtin_amdgcn_update_dpp(0u, __float_as_uint(accQ[0][r]), 0x150, 0xf, 0xf, true));
                ck4[r] = __uint_as_float(__builtin_amdgcn_update_dpp(0u, __float_as_uint(accK[0][r]), 0x150, 0xf, 0xf, true));
            }
            sfence();
            u32x2 qp[5], kp[5], vp[5];
            #pragma unroll
            for (int a = 0; a < 5; a++) {
                f32x4 t = (accK[a] - st[a][0] * ck4) * st[a][1];
                kp[a][0] = pk_bf16(t[0], t[1]); kp[a][1] = pk_bf16(t[2], t[3]);
                if (a > 0) {
                    t = accQ[a] - st[a][0] * cq4;
                    qp[a][0] = pk_bf16(t[0], t[1]); qp[a][1] = pk_bf16(t[2], t[3]);
                }
            }
            f32x4 s1[5], s2[5];
            const f32x4 z = {};
            #pragma unroll
            for (int a = 1; a < 5; a++) { s1[a] = mfma16(kp[a - 1], qp[a], z); s2[a] = mfma16(kp[a], qp[a], z); }
            #pragma unroll
            for (int a = 0; a < 5; a++) {
                const float t0 = (accV[a][0] - s01[a][0] * cv) * s01[a][1], t1 = (accV[a][1] - s01[a][2] * cv) * s01[a][3];
                const float t2 = (accV[a][2] - s23[a][0] * cv) * s23[a][1], t3 = (accV[a][3] - s23[a][2] * cv) * s23[a][3];
                vp[a][0] = pk_bf16(t0, t1); vp[a][1] = pk_bf16(t2, t3);
            }
            sfence();
            float v0[5], v1[5], mx[5], e0[5], e1[5], sum[5];
            #pragma unroll
            for (int a = 1; a < 5; a++) {
                const f32x4 p1 = s1[a], p2 = s2[a];
                v0[a] = sel(m0, sel(par, p1[1], p1[0]), sel(par, p2[1], p2[0]));
                v1[a] = sel(m1, sel(par, p1[3], p1[2]), sel(par, p2[3], p2[2]));
            }
            v0[1] = sel(m0 && rt1 == 0, NEG, v0[1]); v1[1] = sel(m1 && rt1 == 0, NEG, v1[1]);
            #pragma unroll
            for (int a = 1; a < 5; a++) mx[a] = vmax(v0[a], v1[a]);
            #pragma unroll
            for (int a = 1; a < 5; a++) { auto p = __builtin_amdgcn_permlane16_swap(__float_as_uint(mx[a]), __float_as_uint(mx[a]), false, false); mx[a] = vmax(__uint_as_float(p[0]), __uint_as_float(p[1])); }
            #pragma unroll
            for (int a = 1; a < 5; a++) { auto p = __builtin_amdgcn_permlane32_swap(__float_as_uint(mx[a]), __float_as_uint(mx[a]), false, false); mx[a] = vmax(__uint_as_float(p[0]), __uint_as_float(p[1])); }
            #pragma unroll
            for (int a = 1; a < 5; a++) { const float cs_ = CS * st[a][1]; e0[a] = __builtin_amdgcn_exp2f((v0[a] - mx[a]) * cs_); e1[a] = __builtin_amdgcn_exp2f((v1[a] - mx[a]) * cs_); sum[a] = e0[a] + e1[a]; }
            #pragma unroll
            for (int a = 1; a < 5; a++) { auto p = __builtin_amdgcn_permlane16_swap(__float_as_uint(sum[a]), __float_as_uint(sum[a]), false, false); sum[a] = __uint_as_float(p[0]) + __uint_as_float(p[1]); }
            #pragma unroll
            for (int a = 1; a < 5; a++) { auto p = __builtin_amdgcn_permlane32_swap(__float_as_uint(sum[a]), __float_as_uint(sum[a]), false, false); sum[a] = __uint_as_float(p[0]) + __uint_as_float(p[1]); }
            f32x4 o[5];
            #pragma unroll
            for (int a = 1; a < 5; a++) {
                const float inv = __builtin_amdgcn_rcpf(sum[a]);
                const unsigned pk = pk_bf16(e0[a] * inv, e1[a] * inv);
                u32x4 pf, vf;
                pf[0] = __builtin_amdgcn_perm(pk, 0u, sel0); pf[1] = __builtin_amdgcn_perm(pk, 0u, sel1);
                pf[2] = __builtin_amdgcn_perm(pk, 0u, sel2); pf[3] = __builtin_amdgcn_perm(pk, 0u, sel3);
                vf[0] = vp[a - 1][0]; vf[1] = vp[a - 1][1]; vf[2] = vp[a][0]; vf[3] = vp[a][1];
                o[a] = mfma32(vf, pf, z);
            }
            #pragma unroll
            for (int a = 1; a < 5; a++) {
                u32x2 op; op[0] = pk_bf16(o[a][0], o[a][1]); op[1] = pk_bf16(o[a][2], o[a][3]);
                __builtin_amdgcn_raw_buffer_store_b64(op, rs_ws, o_off, (a - 1) * 16384, 16);
            }
        }
        STAMP(2);
        VMCNT(0); barx();
        STAMP(3);
        u32x4 bo[2][16];
        #pragma unroll
        for (int k = 0; k < 16; k++) {
            LAS char* p = sm + (k >> 1) * 8192 + (k & 1) * 2048 + tr_pair;
            bo[0][k] = tr_frag(p); bo[1][k] = tr_frag(p + 8);
        }
        u32x4 b1[10][3];
        f32x4 cs[3] = {};
        u32x4 ones; ones[0] = ONES; ones[1] = ONES; ones[2] = ONES; ones[3] = ONES;
        auto absorb1 = [&](int G) {
            #pragma unroll
            for (int b = 0; b < 2; b++)
                #pragma unroll
                for (int s = 0; s < 3; s++) b1[2 * G + b][s] = tr_frag(sm + L_RING + (G % 3) * 24576 + s * 8192 + b * 2048 + tr_plain);
        };
        auto colsum1 = [&](int G) {
            #pragma unroll
            for (int b = 0; b < 2; b++)
                #pragma unroll
                for (int s = 0; s < 3; s++) cs[s] = mfma32(b1[2 * G + b][s], ones, cs[s]);
        };
        absorb1(0); absorb1(1);
        colsum1(0); colsum1(1);
        sfence();
        STAMP(4);
        bar();
        u32x4 hres[2];
        unsigned h_off = WS_HT + ((rt * 4 + 2 * kh) * 16 + cb32) * 1024 + l * 16; PIN(h_off);
        auto xch_put = [&](f32x4 (&acc)[2][4]) {
            LAS char* xo = sm + L_XCH + j * 4096 + l * 16;
            #pragma unroll
            for (int ab = 0; ab < 2; ab++)
                #pragma unroll
                for (int m = 0; m < 2; m++) *(LAS f32x4*)(xo + (ab * 2 + m) * 1024) = kh ? acc[ab][m] : acc[ab][2 + m];
        };
        auto xch_get = [&](f32x4 (&acc)[2][4], f32x4 (&res)[2][2]) {
            LAS char* xi = sm + L_XCH + (j ^ 2) * 4096 + l * 16;
            #pragma unroll
            for (int ab = 0; ab < 2; ab++)
                #pragma unroll
                for (int m = 0; m < 2; m++) res[ab][m] = (kh ? acc[ab][2 + m] : acc[ab][m]) + *(LAS f32x4*)(xi + (ab * 2 + m) * 1024);
        };
        {
            f32x4 acc[2][2];
            #pragma unroll
            for (int m = 0; m < 2; m++)
                #pragma unroll
                for (int ab = 0; ab < 2; ab++) {
                    acc[ab][m][0] = bf_lo(xr[m][2 * ab]); acc[ab][m][1] = bf_hi(xr[m][2 * ab]);
                    acc[ab][m][2] = bf_lo(xr[m][2 * ab + 1]); acc[ab][m][3] = bf_hi(xr[m][2 * ab + 1]);
                }
            u32x4 a2[2][2][2];
            auto R2 = [&](int k2) {
                #pragma unroll
                for (int k = 0; k < 2; k++)
                    #pragma unroll
                    for (int m = 0; m < 2; m++) a2[k2 & 1][k][m] = lds_r128(sm + (2 * kh + m) * 16384 + (2 * k2 + k) * 1024 + l * 16);
            };
            sfence();
            barx();
            STAMP(5);
            R2(0); sfence();
            #pragma unroll
            for (int k2 = 0; k2 < 8; k2++) {
                if (k2 == 3) barx();
                if (k2 < 7) R2(k2 + 1);
                #pragma unroll
                for (int k = 0; k < 2; k++)
                    #pragma unroll
                    for (int m = 0; m < 2; m++) {
                        acc[0][m] = mfma32(bo[0][2 * k2 + k], a2[k2 & 1][k][m], acc[0][m]);
                        acc[1][m] = mfma32(bo[1][2 * k2 + k], a2[k2 & 1][k][m], acc[1][m]);
                    }
                #pragma unroll
                for (int i = 0; i < 4; i++) { SGB(0x8, 1); SGB(0x100, 1); }
                SGB(0x8, 4);
                sfence();
            }
            STAMP(6);
            #pragma unroll
            for (int m = 0; m < 2; m++) {
                hres[m][0] = pk_bf16(acc[0][m][0], acc[0][m][1]); hres[m][1] = pk_bf16(acc[0][m][2], acc[0][m][3]);
                hres[m][2] = pk_bf16(acc[1][m][0], acc[1][m][1]); hres[m][3] = pk_bf16(acc[1][m][2], acc[1][m][3]);
                __builtin_amdgcn_raw_buffer_store_b128(hres[m], rs_ws, h_off, m * 16384, 16);
                *(LAS u32x4*)(sm + L_XCH + j * 4096 + m * 1024 + l * 16) = hres[m];
            }
        }
        STAMP(7);
        VMCNT(0); bar();
        STAMP(8);
        absorb1(2); absorb1(3); absorb1(4);
        bar();
        colsum1(2); colsum1(3); colsum1(4);
        sfence();
        STAMP(9);
        barx();
        {
            f32x4 acc[3][4] = {};
            u32x4 a3[4], br[2][3];
            float st[4]; unsigned f_off[3];
            auto a3_off = [&](int pos) {
                const int p = pos >> 1;
                return (unsigned)((((p == 0 ? 0 : p < 4 ? p + 4 : p - 3) + urot) & 7) * 2048 + (pos & 1) * 1024);
            };
            barx();
            STAMP(10);
            #pragma unroll
            for (int m = 0; m < 4; m++) a3[m] = lds_r128(sm + m * 16384 + a3_off(0) + l * 16);
            sfence();
            #pragma unroll
            for (int pos = 0; pos < 16; pos++) {
                if ((pos & 3) == 3 && pos < 15) bar();
                const bool res = pos >= 2 && pos < 8, nres = pos >= 1 && pos < 7;
                if (pos == 12) {
                    #pragma unroll
                    for (int m = 0; m < 4; m++) st[m] = *(LAS float*)(sm + L_S3 + (m * 16 + i16) * 8);
                    #pragma unroll
                    for (int s = 0; s < 3; s++) {
                        const int T = mem * 12 + s * 4 + j;
                        f_off[s] = T < 86 ? WS_FT + ((rt * 4 * 43 + (T >> 1)) * 64 + (2 * (T & 1) + (g >> 1)) * 16 + i16) * 16 + (g & 1) * 8 : OOB;
                        PIN(f_off[s]);
                    }
                }
                #pragma unroll
                for (int m = 0; m < 4; m++) {
                    if (nres && m < 3) br[(pos + 1) & 1][m] = tr_frag(sm + L_RING + ((((pos + 1) >> 1) + 4) % 3) * 24576 + m * 8192 + ((pos + 1) & 1) * 2048 + tr_plain);
                    #pragma unroll
                    for (int s = 0; s < 3; s++) {
                        const u32x4 b = res ? br[pos & 1][s] : b1[pos < 2 ? pos : pos < 8 ? 0 : pos - 6][s];
                        acc[s][m] = mfma32(b, a3[m], acc[s][m]);
                    }
                    if (res && m < 3) cs[m] = mfma32(br[pos & 1][m], ones, cs[m]);
                    SGB(0x8, 1); SGB(0x100, 1); SGB(0x8, 1); SGB(0x100, 1); SGB(0x8, 2);
                    sfence();
                    if (pos < 15) a3[m] = lds_r128(sm + m * 16384 + a3_off(pos + 1) + l * 16);
                    sfence();
                }
            }
            STAMP(11);
            barx();
            #pragma unroll
            for (int m = 0; m < 4; m++)
                #pragma unroll
                for (int s = 0; s < 3; s++) {
                    const f32x4 t = acc[s][m] - st[m] * cs[s];
                    u32x2 o; o[0] = relu_pk(pk_bf16(t[0], t[1])); o[1] = relu_pk(pk_bf16(t[2], t[3]));
                    __builtin_amdgcn_raw_buffer_store_b64(o, rs_ws, f_off[s], m * (43 * 1024), 16);
                }
        }
        STAMP(12);
        VMCNT(0); barx();
        STAMP(13);
        {
            u32x4 bw[NA][2];
            float rowsc[2];
            auto absorb2 = [&](int G) {
                #pragma unroll
                for (int u = 0; u < 3; u++) {
                    LAS char* p = sm + L_RING + ((G + 2) % 3) * 24576 + u * 8192 + kh * 2048 + tr_pair;
                    bw[3 * G + u][0] = tr_frag(p); bw[3 * G + u][1] = tr_frag(p + 8);
                }
            };
            absorb2(0); absorb2(1); absorb2(2);
            #pragma unroll
            for (int m = 0; m < 2; m++) { hres[m] = lds_r128(sm + L_XCH + j * 4096 + m * 1024 + l * 16); PIN(hres[m]); rowsc[m] = *(LAS float*)(sm + L_S3 + ((2 * kh + m) * 16 + i16) * 8 + 4); PIN(rowsc[m]); }
            bar();
            barx();
            absorb2(3); absorb2(4); absorb2(5);
            STAMP(14);
            f32x4 acc[2][4] = {};
            u32x4 a4[2][4], br[2][2];
            auto R4 = [&](int u) {
                #pragma unroll
                for (int m = 0; m < 4; m++) a4[u & 1][m] = lds_r128(sm + ((u >> 1) & 3) * 16384 + m * 4096 + kh * 2048 + (u & 1) * 1024 + l * 16);
                if (u >= NA) {
                    LAS char* p = sm + L_RING + ((u / 3 + 2) % 3) * 24576 + (u % 3) * 8192 + kh * 2048 + tr_pair;
                    br[u & 1][0] = tr_frag(p); br[u & 1][1] = tr_frag(p + 8);
                }
            };
            bar();
            STAMP(15);
            R4(0); sfence();
            #pragma unroll
            for (int u = 0; u < 22; u++) {
                if ((u & 1) && u < 21) bar();
                if (u < 21) R4(u + 1);
                #pragma unroll
                for (int ab = 0; ab < 2; ab++) {
                    const u32x4 b = u < NA ? bw[u < NA ? u : 0][ab] : br[u & 1][ab];
                    #pragma unroll
                    for (int m = 0; m < 4; m++) acc[ab][m] = mfma32(b, a4[u & 1][m], acc[ab][m]);
                }
                #pragma unroll
                for (int i = 0; i < 8; i++) { SGB(0x8, 1); SGB(0x100, 1); }
                sfence();
            }
            STAMP(24);
            xch_put(acc);
            bar();
            f32x4 r[2][2];
            xch_get(acc, r);
            #pragma unroll
            for (int m = 0; m < 2; m++) {
                u32x4 o;
                o[0] = pk_bf16(bf_lo(hres[m][0]) + rowsc[m] * r[0][m][0], bf_hi(hres[m][0]) + rowsc[m] * r[0][m][1]);
                o[1] = pk_bf16(bf_lo(hres[m][1]) + rowsc[m] * r[0][m][2], bf_hi(hres[m][1]) + rowsc[m] * r[0][m][3]);
                o[2] = pk_bf16(bf_lo(hres[m][2]) + rowsc[m] * r[1][m][0], bf_hi(hres[m][2]) + rowsc[m] * r[1][m][1]);
                o[3] = pk_bf16(bf_lo(hres[m][3]) + rowsc[m] * r[1][m][2], bf_hi(hres[m][3]) + rowsc[m] * r[1][m][3]);
                *(u32x4*)(out + ((rt * 4 + 2 * kh + m) * 16 + i16) * 512 + cb32 * 32 + 8 * g) = o;
            }
        }
        STAMP(25);
    }
}

extern "C" void kernel_launch(void* const* d_in, const int* in_sizes, int n_in, void* d_out, int out_size, void* d_ws, size_t ws_size, hipStream_t stream) {
    (void)in_sizes; (void)n_in; (void)out_size; (void)ws_size;
    (void)hipFuncSetAttribute((const void*)fused_block_kernel, hipFuncAttributeMaxDynamicSharedMemorySize, LDS_BYTES);
    fused_block_kernel<<<256, 512, LDS_BYTES, stream>>>((const u16*)d_in[0], (const u16*)d_in[1], (const u16*)d_in[2], (const u16*)d_in[4], (const u16*)d_in[5], (u16*)d_out, (char*)d_ws);
}
```

```cpp
#include <hip/hip_runtime.h>
#include <hip/hip_bf16.h>
#include <stdint.h>

typedef unsigned short u16;
typedef unsigned long long u64;
typedef __attribute__((ext_vector_type(8))) short s16x8;
typedef __attribute__((ext_vector_type(4))) short s16x4;
typedef __attribute__((ext_vector_type(4))) float f32x4;
typedef __attribute__((ext_vector_type(2))) float f32x2;
typedef __attribute__((ext_vector_type(4))) unsigned u32x4;
typedef __attribute__((ext_vector_type(2))) unsigned u32x2;
typedef __attribute__((ext_vector_type(2))) __bf16 bf16x2;
#define GAS __attribute__((address_space(1)))
#define LAS __attribute__((address_space(3)))
typedef GAS unsigned gu32;
#define RLX_AGENT __ATOMIC_RELAXED, __HIP_MEMORY_SCOPE_AGENT
#define DEV __device__ __forceinline__

namespace {
constexpr int S_ = 2048, D_ = 512, DFF = 1376;
constexpr unsigned WS_EPOCH = 0;
constexpr unsigned WS_SFLAG = 524288;
constexpr unsigned WS_ST1 = 131072;
constexpr unsigned WS_ST2 = 262144;
constexpr unsigned WS_OT  = 1u << 20;
constexpr unsigned WS_HT  = 3u << 20;
constexpr unsigned WS_FT  = 5u << 20;
constexpr unsigned WS_USED = 11u << 20;
constexpr unsigned L_SLOT = 34816;
constexpr unsigned L_RING = 69632;
constexpr unsigned L_XCH = 143360;
constexpr unsigned L_CTL = 159744;
constexpr unsigned L_FL = L_CTL;
constexpr unsigned L_ST = L_CTL + 256;
constexpr unsigned L_S3 = L_CTL + 1024;
constexpr unsigned LDS_BYTES = 163840;
constexpr unsigned SPIN_CAP = 1u << 18;
constexpr int CODE_KB = 28;
constexpr int NA = 13;
constexpr int NH = 7;

DEV float bf_lo(unsigned v) { return __uint_as_float(v << 16); }
DEV float bf_hi(unsigned v) { return __uint_as_float(v & 0xffff0000u); }
DEV unsigned pk_bf16(float a, float b) { bf16x2 r; r[0] = (__bf16)a; r[1] = (__bf16)b; return __builtin_bit_cast(unsigned, r); }
DEV float rbf(float a) { return (float)((__bf16)a); }
DEV float dot2(unsigned a, unsigned b, float c) {
    return __builtin_amdgcn_fdot2_f32_bf16(__builtin_bit_cast(bf16x2, a), __builtin_bit_cast(bf16x2, b), c, false);
}
DEV f32x4 mfma32(u32x4 a, u32x4 b, f32x4 c) {
    return __builtin_amdgcn_mfma_f32_16x16x32_bf16(__builtin_bit_cast(s16x8, a), __builtin_bit_cast(s16x8, b), c, 0, 0, 0);
}
DEV f32x4 mfma16(u32x2 a, u32x2 b, f32x4 c) {
    return __builtin_amdgcn_mfma_f32_16x16x16bf16_1k(__builtin_bit_cast(s16x4, a), __builtin_bit_cast(s16x4, b), c, 0, 0, 0);
}
#define MFMA_IP(acc, a, b) do { asm volatile("v_mfma_f32_16x16x32_bf16 %0, %1, %2, %0" : "+v"(acc) : "v"(a), "v"(b)); __builtin_amdgcn_sched_barrier(0); } while (0)
#define MFMA_END() do { asm volatile("s_nop 7\n\ts_nop 3" ::: "memory"); __builtin_amdgcn_sched_barrier(0); } while (0)
DEV u32x2 tr_read(LAS char* p) { return __builtin_bit_cast(u32x2, __builtin_amdgcn_ds_read_tr16_b64_v4i16((LAS s16x4*)p)); }
DEV u32x4 tr_frag(LAS char* p) { u32x2 lo = tr_read(p), hi = tr_read(p + 512); u32x4 v; v[0] = lo[0]; v[1] = lo[1]; v[2] = hi[0]; v[3] = hi[1]; return v; }
DEV u32x4 lds_r128(LAS char* p) { return *(LAS u32x4*)p; }
template <int CTRL> DEV float dpp_add(float v) {
    return v + __uint_as_float(__builtin_amdgcn_update_dpp(0, __float_as_uint(v), CTRL, 0xf, 0xf, true));
}
DEV f32x2 ln_stats(float s, float q) {
    float m = s * (1.0f / 512.0f);
    float var = fmaxf(q * (1.0f / 512.0f) - m * m, 0.0f);
    float sd = rbf(__builtin_amdgcn_sqrtf(rbf(rbf(var) + 1e-5f)));
    f32x2 r; r[0] = rbf(m); r[1] = sd; return r;
}
DEV unsigned ln_pack(float s, float q) {
    float m = s * (1.0f / 512.0f);
    float var = fmaxf(q * (1.0f / 512.0f) - m * m, 0.0f);
    return pk_bf16(m, __builtin_amdgcn_sqrtf(rbf(rbf(var) + 1e-5f)));
}
typedef __attribute__((ext_vector_type(2))) short s16x2;
DEV unsigned cvt_pk(float a, float b) { unsigned r; asm("v_cvt_pk_bf16_f32 %0, %1, %2" : "=v"(r) : "v"(a), "v"(b)); return r; }
DEV unsigned relu_pk(unsigned v) { const s16x2 z = {0, 0}; return __builtin_bit_cast(unsigned, __builtin_elementwise_max(__builtin_bit_cast(s16x2, v), z)); }
DEV float tsum4(const float (&v)[4]) {
    auto a = __builtin_amdgcn_permlane16_swap(__float_as_uint(v[0]), __float_as_uint(v[1]), false, false);
    auto b = __builtin_amdgcn_permlane16_swap(__float_as_uint(v[2]), __float_as_uint(v[3]), false, false);
    const float u = __uint_as_float(a[0]) + __uint_as_float(a[1]), w = __uint_as_float(b[0]) + __uint_as_float(b[1]);
    auto c = __builtin_amdgcn_permlane32_swap(__float_as_uint(u), __float_as_uint(w), false, false);
    return __uint_as_float(c[0]) + __uint_as_float(c[1]);
}
DEV float sel(bool c, float a, float b) { return c ? a : b; }
DEV void fence() { asm volatile("" ::: "memory"); }
DEV void sfence() { asm volatile("" ::: "memory"); __builtin_amdgcn_sched_barrier(0); }
#define VMCNT(n) asm volatile("s_waitcnt vmcnt(" #n ")" ::: "memory")
#define SGB(mask, n) __builtin_amdgcn_sched_group_barrier(mask, n, 0)
#define PIN(x) asm volatile("" : "+v"(x))
#define RFL(x) ((unsigned)__builtin_amdgcn_readfirstlane(x))
#define DMA(rs, ldsoff, voff, soff, imm, aux) __builtin_amdgcn_raw_ptr_buffer_load_lds(rs, (LAS void*)(sm + (ldsoff)), 16, voff, soff, imm, aux)
#define DMA4(rs, ldsoff, vo, soff, aux) do { const unsigned lo_ = (ldsoff), so_ = (soff); DMA(rs, lo_, vo[0], so_, 0, aux); DMA(rs, lo_, vo[1], so_, 1024, aux); \
    DMA(rs, lo_, vo[2], so_, 2048, aux); DMA(rs, lo_, vo[3], so_, 3072, aux); } while (0)
DEV void bar() { asm volatile("s_waitcnt lgkmcnt(0)" ::: "memory"); __builtin_amdgcn_s_barrier(); asm volatile("" ::: "memory"); }
DEV void barx() { asm volatile("" ::: "memory"); __builtin_amdgcn_s_barrier(); asm volatile("" ::: "memory"); }
}

__global__ __launch_bounds__(512, 2) void fused_block_kernel(const u16* __restrict__ x, const u16* __restrict__ wqkv, const u16* __restrict__ wo,
                                                            const u16* __restrict__ w1, const u16* __restrict__ w2, u16* __restrict__ out, char* ws) {
    extern __shared__ __attribute__((aligned(16))) char smem_[];
    LAS char* sm = (LAS char*)smem_;
    const int bid = blockIdx.x, tid = threadIdx.x;
    unsigned cpf = 0;
    {
        u64 kbase; asm volatile("s_getpc_b64 %0" : "=s"(kbase));
        if (tid < CODE_KB * 8) asm volatile("global_load_dword %0, %1, %2" : "=v"(cpf) : "v"(tid * 128), "s"(kbase) : "memory");
    }
    const int w = __builtin_amdgcn_readfirstlane(tid >> 6), l = tid & 63;
    const int g = l >> 4, i16 = l & 15;
    const int xcd = bid & 7, idx = bid >> 3;
    const int rt = (xcd >> 1) * 8 + (idx >> 2), mem = (xcd & 1) * 4 + (idx & 3);
    const int urot = (mem & 3) * 2, frot = (mem * 3) >> 1;
    const int crot = ((rt & 7) + 2 * (mem & 3)) & 7;
    const int r0 = rt * 64;
    const int row8 = l >> 3, ch = l & 7;
    const auto rs_x = __builtin_amdgcn_make_buffer_rsrc((void*)x, 0, S_ * D_ * 2, 0x00020000);
    const auto rs_ws = __builtin_amdgcn_make_buffer_rsrc((void*)ws, 0, WS_USED, 0x00020000);
    const unsigned OOB = 0x80000000u;
    const unsigned ONES = 0x3f803f80u;

    unsigned tagr = 0;
    if (w >= 6) {
        const int hw = w - 6;
        u32x4 xv[8];
        int ar = r0 - 16 + mem * 10 + hw * 5 + row8; ar = ar < 0 ? 0 : ar;
        const unsigned vo = l < 40 ? (unsigned)(ar * 1024 + ch * 16) : OOB;
        #pragma unroll
        for (int i = 0; i < 8; i++) xv[i] = __builtin_amdgcn_raw_buffer_load_b128(rs_x, vo + i * 128, 0, 0);
        const unsigned tag = __hip_atomic_load((gu32*)(ws + WS_EPOCH + rt * 256), RLX_AGENT) + 1u;
        if (hw == 0 && l < 8) *(LAS unsigned*)(sm + L_FL + l * 4) = 0u;
        if (hw == 1 && l < 32) { u32x4 o; o[0] = ONES; o[1] = ONES; o[2] = ONES; o[3] = ONES; *(LAS u32x4*)(sm + (l >> 3) * L_SLOT + (l & 7) * 16) = o; }
        sfence();
        float ss[2] = {}, qq[2] = {};
        #pragma unroll
        for (int i = 0; i < 8; i++)
            #pragma unroll
            for (int j = 0; j < 4; j++) { ss[j & 1] = dot2(xv[i][j], ONES, ss[j & 1]); qq[j & 1] = dot2(xv[i][j], xv[i][j], qq[j & 1]); }
        tagr = RFL(tag);
        float s = ss[0] + ss[1], q = qq[0] + qq[1];
        s = dpp_add<0xB1>(s); q = dpp_add<0xB1>(q); s = dpp_add<0x4E>(s); q = dpp_add<0x4E>(q); s = dpp_add<0x141>(s); q = dpp_add<0x141>(q);
        u32x2 gr; gr[0] = ln_pack(s, q); gr[1] = tagr;
        __builtin_amdgcn_raw_buffer_store_b64(gr, rs_ws, (ch == 0 && l < 40) ? WS_ST1 + (rt * 80 + mem * 10 + hw * 5 + row8) * 8 : OOB, 0, 16);
    }
    sfence();
    if (w == 4 || w == 5) {
        int l_ = tid & 63; asm volatile("" : "+v"(l_));
        const int l = l_, row8 = l >> 3, ch = l & 7;
        const int lw = w - 4;
        const auto rs_wqkv = __builtin_amdgcn_make_buffer_rsrc((void*)wqkv, 0, D_ * 3 * D_ * 2, 0x00020000);
        unsigned a_vo[5], q_vo[4];
        #pragma unroll
        for (int e = 0; e < 5; e++) {
            int ar = r0 - 16 + (lw * 5 + e) * 8 + row8; ar = ar < 0 ? 0 : ar;
            a_vo[e] = (ar * 512 + ((ch ^ row8) * 8)) * 2 + 4096 - (e ? (e - 1) * 1024 : 0);
        }
        const auto rs_xm = __builtin_amdgcn_make_buffer_rsrc((void*)((const char*)x - 4096), 0, S_ * D_ * 2 + 4096, 0x00020000);
        const unsigned swz = (ch ^ ((((row8 >> 1) & 1) | (lw << 1)) << 1)) * 16;
        #pragma unroll
        for (int i = 0; i < 4; i++) q_vo[i] = (lw * 8 + i * 16 + row8) * 3072 + mem * 128 + swz - i * 1024;
        auto P1 = [&](int c) {
            const int cc = (c + crot) & 7;
            const unsigned sl = (c & 3) * L_SLOT;
            unsigned so = cc * 128; asm volatile("" : "+s"(so));
            DMA4(rs_xm, sl + lw * 5120 + 1024, (a_vo + 1), so, 0);
            if (lw == 1 || l >= 8) DMA(rs_xm, sl + lw * 5120, a_vo[0], so, 0, 0);
            fence();
            #pragma unroll
            for (int s = 0; s < 3; s++) DMA4(rs_wqkv, sl + 10240 + s * 8192 + lw * 4096, q_vo, cc * (64 * 3072) + s * 1024, 0);
        };
        P1(0); P1(1);
        sfence();
        const auto rs_wo = __builtin_amdgcn_make_buffer_rsrc((void*)wo, 0, D_ * D_ * 2, 0x00020000);
        const auto rs_w1 = __builtin_amdgcn_make_buffer_rsrc((void*)w1, 0, D_ * DFF * 2, 0x00020000);
        const auto rs_w2 = __builtin_amdgcn_make_buffer_rsrc((void*)w2, 0, D_ * DFF * 2, 0x00020000);
        unsigned wo_vo[4], w1_vo[4], w2_vo[4], lin_vo, f_vo[4];
        #pragma unroll
        for (int i = 0; i < 4; i++) {
            wo_vo[i] = (lw * 8 + i * 16 + row8) * 1024 + mem * 128 + swz - i * 1024;
            w1_vo[i] = (lw * 8 + i * 16 + row8) * (DFF * 2) + mem * 384 + swz - i * 1024;
            w2_vo[i] = (lw * 8 + (i & 1) * 16 + (i >> 1) * 704 + row8) * 1024 + mem * 128 + swz - i * 1024;
            f_vo[i] = l * 16 + (i >> 1) * (20 * 1024);
            PIN(wo_vo[i]); PIN(w1_vo[i]); PIN(w2_vo[i]);
        }
        lin_vo = l * 16;
        asm volatile("" :: "s"(wo), "s"(w1), "s"(w2));
        auto WO = [&](int G) {
            #pragma unroll
            for (int i = 0; i < 4; i++) { const unsigned u = (G * 4 + i + urot) & 7; DMA4(rs_wo, (G * 4 + i) * 8192 + lw * 4096, wo_vo, u * 65536, 0); }
        };
        auto W1 = [&](int G) {
            const unsigned u = (G + mem) & 7;
            #pragma unroll
            for (int s = 0; s < 3; s++) DMA4(rs_w1, L_RING + (G % 3) * 24576 + s * 8192 + lw * 4096, w1_vo, u * (64 * DFF * 2) + s * 128, 0);
        };
        auto U2 = [&](int p) {
            unsigned ua = p + 2 * frot; ua = ua >= 22 ? ua - 22 : ua;
            DMA4(rs_w2, L_RING + (p - NA) * 8192 + lw * 4096, w2_vo, ua * 32768, 0);
        };
        unsigned t_so[4];
        #pragma unroll
        for (int q = 0; q < 4; q++) { t_so[q] = (rt * 4 + lw) * 16384 + ((2 * q + urot) & 7) * 2048; asm volatile("" : "+s"(t_so[q])); }
        const unsigned lin4[4] = {lin_vo, lin_vo, lin_vo, lin_vo};
        auto TILE = [&](unsigned base) {
            #pragma unroll
            for (int q = 0; q < 4; q++)
                #pragma unroll
                for (int m = 0; m < 2; m++) DMA4(rs_ws, lw * 16384 + m * 32768 + q * 4096, lin4, base + m * 32768 + t_so[q], 16);
        };
        unsigned h_so[7];
        #pragma unroll
        for (int p = 0; p < 7; p++) { h_so[p] = WS_HT + (rt * 4 + lw) * 16384 + ((p + 1 + mem) & 7) * 2048; asm volatile("" : "+s"(h_so[p])); }
        auto TILE_H = [&]() {
            #pragma unroll
            for (int p = 0; p < 7; p++)
                #pragma unroll
                for (int m = 0; m < 2; m++) {
                    DMA(rs_ws, lw * 16384 + m * 32768 + (p + 1) * 2048, lin_vo, h_so[p] + m * 32768, 0, 16);
                    DMA(rs_ws, lw * 16384 + m * 32768 + (p + 1) * 2048, lin_vo, h_so[p] + m * 32768, 1024, 16);
                }
        };
        auto prog = [&](unsigned v) { fence(); *(volatile LAS unsigned*)(sm + L_FL + lw * 4) = v; fence(); };
        VMCNT(17);
        barx();
        #pragma nounroll
        for (int r = 0; r < 6; r++) { P1(r + 2); VMCNT(17); barx(); }
        WO(0); VMCNT(16); barx();
        WO(1); W1(0); barx();
        W1(1); VMCNT(12); barx();
        __builtin_amdgcn_s_sleep(2);
        W1(2); VMCNT(12); barx();
        TILE(WS_OT);
        VMCNT(16); barx();
        W1(3); VMCNT(12); prog(1);
        W1(4);
        VMCNT(0); barx();
        barx();
        __builtin_amdgcn_s_sleep(2);
        W1(5);
        barx();
        TILE_H();
        VMCNT(16); barx();
        W1(6); VMCNT(12); prog(2);
        W1(7); VMCNT(12); prog(3);
        {
            u32x4 sv[NA - NH][4];
            #pragma unroll
            for (int p = NH; p < NA; p++) {
                unsigned ua = p + 2 * frot; ua = ua >= 22 ? ua - 22 : ua;
                #pragma unroll
                for (int i = 0; i < 4; i++) sv[p - NH][i] = __builtin_amdgcn_raw_buffer_load_b128(rs_w2, w2_vo[i] + i * 1024, ua * 32768, 0);
            }
            sfence();
            VMCNT(24); prog(4);
            barx();
            auto dump = [&](int p0, int p1) {
                #pragma unroll
                for (int p = p0; p < p1; p++)
                    #pragma unroll
                    for (int i = 0; i < 4; i++) *(LAS u32x4*)(sm + (p < 8 ? p * 8192 : L_RING + (p - 8) * 8192) + lw * 4096 + i * 1024 + l * 16) = sv[p - NH][i];
                sfence();
            };
            dump(NH, NA);
            U2(18); U2(19); U2(20); U2(21);
            bar();
        }
        #pragma nounroll
        for (unsigned s = 0; s < SPIN_CAP; s++) {
            const u32x4 v = *(volatile LAS u32x4*)(sm + L_FL + 16);
            if (RFL(v[0] & v[1] & v[2] & v[3]) != 0u) break;
        }
        fence();
        U2(13); U2(14); U2(15); U2(16); U2(17);
        unsigned fsrc = WS_FT + (rt * 4 + lw) * (43 * 1024); asm volatile("" : "+s"(fsrc));
        PIN(f_vo[0]); PIN(f_vo[2]);
        barx();
        auto FG = [&](unsigned F) {
            unsigned Fa = F + frot; Fa = Fa >= 11 ? Fa - 11 : Fa;
            f_vo[3] = Fa == 10 ? OOB : f_vo[2];
            #pragma unroll
            for (int m = 0; m < 2; m++) DMA4(rs_ws, ((F == 4 || F == 9) ? L_XCH : (F >= 10 ? F - 10 : F >= 5 ? F - 5 : F) * 16384) + (2 * m + lw) * 4096, f_vo, fsrc + m * (86 * 1024) + Fa * 2048, 16);
        };
        #pragma unroll
        for (unsigned F = 0; F < 4; F++) FG(F);
        VMCNT(24); barx();
        FG(4); VMCNT(24); barx();
        VMCNT(16); barx();
        #pragma unroll
        for (unsigned F = 5; F < 11; F++) { FG(F); VMCNT(16); barx(); }
        VMCNT(8); barx();
        VMCNT(0); barx();
        barx();
    } else if (w >= 6) {
        const int hw = w - 6;
        unsigned pf0 = 0, pf1;
        if (hw == 1 && mem == 0) pf0 = __hip_atomic_load((gu32*)(ws + WS_SFLAG + ((l < 3 ? l : 0) * 32 + rt) * 256 + 64), RLX_AGENT);
        sfence();
        bar();
        barx(); barx();
        const unsigned g1_off = l < 40 ? WS_ST1 + (rt * 80 + hw * 40 + l) * 8 : OOB;
        u32x2 gr = __builtin_amdgcn_raw_buffer_load_b64(rs_ws, g1_off, 0, 16);
        sfence();
        barx(); barx();
        #pragma nounroll
        for (unsigned s = 0; s < SPIN_CAP; s++) {
            if (__all(l >= 40 || gr[1] == tagr)) break;
            __builtin_amdgcn_s_sleep(1);
            gr = __builtin_amdgcn_raw_buffer_load_b64(rs_ws, g1_off, 0, 16);
        }
        if (l < 40) { *(LAS float*)(sm + L_ST + (hw * 40 + l) * 4) = bf_lo(gr[0]); *(LAS float*)(sm + L_ST + 320 + (hw * 40 + l) * 4) = __builtin_amdgcn_rcpf(bf_hi(gr[0])); }
        asm volatile("" :: "v"(pf0));
        bar(); barx();
        {
            const char* p = hw ? (const char*)w2 + (bid * 43 + (l < 43 ? l : 0)) * 128
                               : (l < 43 ? (const char*)w1 + (bid * 43 + l) * 128 : (const char*)wo + (bid * 16 + (l & 15)) * 128);
            pf1 = *(const GAS unsigned*)p;
            sfence();
        }
        barx();
        auto signal = [&](int seam) {
            if (hw == 1 && l == 0) __hip_atomic_store((gu32*)(ws + WS_SFLAG + (seam * 32 + rt) * 256 + mem * 4), tagr, RLX_AGENT);
        };
        auto poll = [&](int seam) {
            if (hw == 0) {
                const gu32* fp = (const gu32*)(ws + WS_SFLAG + (seam * 32 + rt) * 256 + (l & 7) * 4);
                __builtin_amdgcn_s_sleep(16);
                #pragma nounroll
                for (unsigned s = 0; s < SPIN_CAP; s++) {
                    const unsigned v = __hip_atomic_load(fp, RLX_AGENT);
                    if (__all(v == tagr)) break;
                    __builtin_amdgcn_s_sleep(1);
                }
            }
        };
        barx();
        asm volatile("" :: "v"(pf1));
        barx();
        signal(0);
        poll(0);
        barx(); barx();
        barx();
        if (hw == 1) {
            u32x4 v[4][2];
            #pragma unroll
            for (int m = 0; m < 4; m++)
                #pragma unroll
                for (int n = 0; n < 2; n++) v[m][n] = lds_r128(sm + L_XCH + ((m >> 1) * 2 + n) * 4096 + (m & 1) * 1024 + l * 16);
            float s2[4] = {}, q2[4] = {};
            #pragma unroll
            for (int j = 0; j < 4; j++)
                #pragma unroll
                for (int m = 0; m < 4; m++) { s2[m] = dot2(v[m][0][j], ONES, s2[m]); q2[m] = dot2(v[m][0][j], v[m][0][j], q2[m]); }
            #pragma unroll
            for (int m = 0; m < 4; m++) { PIN(s2[m]); PIN(q2[m]); }
            sfence();
            barx();
            signal(1);
            sfence();
            #pragma unroll
            for (int j = 0; j < 4; j++)
                #pragma unroll
                for (int m = 0; m < 4; m++) { s2[m] = dot2(v[m][1][j], ONES, s2[m]); q2[m] = dot2(v[m][1][j], v[m][1][j], q2[m]); }
            u32x4 o; o[0] = __float_as_uint(tsum4(s2)); o[1] = tagr; o[2] = __float_as_uint(tsum4(q2)); o[3] = tagr;
            __builtin_amdgcn_raw_buffer_store_b128(o, rs_ws, WS_ST2 + ((rt * 8 + mem) * 64 + l) * 16, 0, 16);
            sfence();
            barx(); barx();
            #pragma nounroll
            for (unsigned s = 0; s < SPIN_CAP; s++) {
                u32x4 p[8];
                #pragma unroll
                for (int i = 0; i < 8; i++) p[i] = __builtin_amdgcn_raw_buffer_load_b128(rs_ws, WS_ST2 + (rt * 512 + l) * 16 + i * 1024, 0, 16);
                bool ok = true; float sx = 0.f, qx = 0.f;
                #pragma unroll
                for (int i = 0; i < 8; i++) { ok = ok && p[i][1] == tagr && p[i][3] == tagr; sx += __uint_as_float(p[i][0]); qx += __uint_as_float(p[i][2]); }
                *(LAS f32x2*)(sm + L_S3 + l * 8) = ln_stats(sx, qx);
                if (__all(ok)) break;
                __builtin_amdgcn_s_sleep(1);
            }
        } else {
            barx();
            poll(1);
            barx();
            if (mem == 0 && l == 0) __hip_atomic_store((gu32*)(ws + WS_EPOCH + rt * 256), tagr, RLX_AGENT);
            barx();
            #pragma nounroll
            for (unsigned s = 0; s < SPIN_CAP; s++) {
                const u32x2 v = *(volatile LAS u32x2*)(sm + L_FL);
                if (RFL(v[0] < v[1] ? v[0] : v[1]) >= 2u) break;
                __builtin_amdgcn_s_sleep(2);
            }
        }
        {
            const auto rs_w2 = __builtin_amdgcn_make_buffer_rsrc((void*)w2, 0, D_ * DFF * 2, 0x00020000);
            const unsigned vo = (hw * 8 + row8) * 1024 + mem * 128 + (ch ^ ((((row8 >> 1) & 1) | (hw << 1)) << 1)) * 16;
            u32x4 sv[NH][4];
            #pragma unroll
            for (int p = 0; p < NH; p++) {
                unsigned ua = p + 2 * frot; ua = ua >= 22 ? ua - 22 : ua;
                #pragma unroll
                for (int i = 0; i < 4; i++) sv[p][i] = __builtin_amdgcn_raw_buffer_load_b128(rs_w2, vo + ((i & 1) * 16 + (i >> 1) * 704) * 1024, ua * 32768, 0);
            }
            sfence();
            bar();
            auto dump = [&](int p0, int p1) {
                #pragma unroll
                for (int p = p0; p < p1; p++)
                    #pragma unroll
                    for (int i = 0; i < 4; i++) *(LAS u32x4*)(sm + (p < 8 ? p * 8192 : L_RING + (p - 8) * 8192) + hw * 4096 + i * 1024 + l * 16) = sv[p][i];
                sfence();
            };
            dump(0, NH);
        }
        bar();
        signal(2);
        poll(2);
        #pragma nounroll
        for (int i = 0; i < 13; i++) barx();
    } else {
        const int j = w;
        const int n2 = j & 1, kh = j >> 1;
        const int q4 = i16 >> 2, p4 = l & 3;
        const unsigned sw_r = ((q4 >> 1) | ((g & 1) << 1)) << 1;
        const unsigned tr_row = ((g & 1) * 4 + (g >> 1)) * 1024 + q4 * 128;
        const unsigned tr_plain = tr_row + (((2 * j + (p4 >> 1)) ^ sw_r) * 16) + (p4 & 1) * 8;
        const unsigned tr_pair = tr_row + (((4 * n2 + p4) ^ sw_r) * 16);
        const int cb32 = mem * 2 + n2;
        u32x2 fl;
        auto fl_read = [&]() { fl = *(volatile LAS u32x2*)(sm + L_FL); };
        auto fl_wait = [&](unsigned need) {
            if (__builtin_expect(RFL(fl[0] < fl[1] ? fl[0] : fl[1]) < need, 0)) {
                #pragma nounroll
                for (unsigned s = 0; s < SPIN_CAP; s++) {
                    const u32x2 v = *(volatile LAS u32x2*)(sm + L_FL);
                    if (RFL(v[0] < v[1] ? v[0] : v[1]) >= need) break;
                }
            }
            sfence();
        };
        u32x4 xr[2] = {};
        {
            const unsigned a_rd0 = i16 * 128 + ((g ^ (i16 & 7)) * 16);
            const unsigned a_rd1 = a_rd0 ^ 64;
            f32x4 accQ[5] = {}, accK[5] = {}, accV[5] = {};
            u32x4 af[2][5], bf[2][3];
            auto R = [&](int s) {
                LAS char* sl = sm + ((s >> 1) & 3) * L_SLOT;
                #pragma unroll
                for (int t = 0; t < 3; t++) bf[s & 1][t] = tr_frag(sl + 10240 + tr_plain + t * 8192 + (s & 1) * 2048);
                #pragma unroll
                for (int t = 0; t < 5; t++) af[s & 1][t] = lds_r128(sl + ((s & 1) ? a_rd1 : a_rd0) + t * 2048);
            };
            auto M = [&](int s) {
                #pragma unroll
                for (int a = 0; a < 5; a++) {
                    accQ[a] = mfma32(bf[s & 1][0], af[s & 1][a], accQ[a]);
                    accK[a] = mfma32(bf[s & 1][1], af[s & 1][a], accK[a]);
                    accV[a] = mfma32(af[s & 1][a], bf[s & 1][2], accV[a]);
                }
            };
            auto RM = [&](int sr) {
                R(sr); M(sr - 1);
                #pragma unroll
                for (int i = 0; i < 11; i++) { SGB(0x8, 1); SGB(0x100, 1); }
                SGB(0x8, 4);
                sfence();
            };
            const float NEG = -1e30f, CS = 0.25f * 1.44269504088896f;
            const int head = mem * 4 + j;
            const bool par = (i16 & 1) != 0;
            const bool m0 = 4 * g + (i16 & 1) >= i16 + 2, m1 = 4 * g + 2 + (i16 & 1) >= i16 + 2;
            const unsigned sP0 = par ? 0x05040c0cu : 0x0c0c0504u, sP1 = par ? 0x07060c0cu : 0x0c0c0706u, sZ = 0x0c0c0c0cu;
            unsigned sel0 = m0 ? sP0 : sZ, sel1 = m1 ? sP1 : sZ, sel2 = m0 ? sZ : sP0, sel3 = m1 ? sZ : sP1;
            unsigned o_off = WS_OT + ((rt * 64 + (head >> 1)) * 64 + (2 * (head & 1) + (g >> 1)) * 16 + i16) * 16 + (g & 1) * 8;
            PIN(sel0); PIN(sel1); PIN(sel2); PIN(sel3); PIN(o_off);
            asm volatile("s_waitcnt vmcnt(0)" : "+v"(cpf) :: "memory");
            float sm_[5], sr_[5]; f32x4 vm[5], vr[5];
            barx();

            R(0); sfence();
            #pragma unroll
            for (int c = 0; c < 8; c++) {
                RM(2 * c + 1);
                if (((c + crot) & 7) == mem) {
                    #pragma unroll
                    for (int m = 0; m < 2; m++) xr[m] = lds_r128(sm + (c & 3) * L_SLOT + (1 + 2 * kh + m) * 2048 + (n2 ? a_rd1 : a_rd0));
                }
                if (c < 7) { bar(); sfence(); RM(2 * c + 2); }
                else {
                    #pragma unroll
                    for (int a = 0; a < 5; a++) {
                        sm_[a] = *(LAS float*)(sm + L_ST + (a * 16 + i16) * 4); sr_[a] = *(LAS float*)(sm + L_ST + 320 + (a * 16 + i16) * 4);
                        vm[a] = *(LAS f32x4*)(sm + L_ST + (a * 16 + 4 * g) * 4); vr[a] = *(LAS f32x4*)(sm + L_ST + 320 + (a * 16 + 4 * g) * 4);
                    }
                    M(15);
                    #pragma unroll
                    for (int i = 0; i < 15; i++) { SGB(0x8, 1); SGB(0x100, 1); }
                    sfence();
                }
            }

            const float cv = __shfl(accV[0][0], i16);
            f32x4 cq4, ck4;
            #pragma unroll
            for (int r = 0; r < 4; r++) {
                cq4[r] = __uint_as_float(__builtin_amdgcn_update_dpp(0u, __float_as_uint(accQ[0][r]), 0x150, 0xf, 0xf, true));
                ck4[r] = __uint_as_float(__builtin_amdgcn_update_dpp(0u, __float_as_uint(accK[0][r]), 0x150, 0xf, 0xf, true));
            }
            u32x2 qp[5], kp[5], vp[5];
            #pragma unroll
            for (int a = 0; a < 5; a++) {
                f32x4 t = (accK[a] - sm_[a] * ck4) * sr_[a];
                kp[a][0] = pk_bf16(t[0], t[1]); kp[a][1] = pk_bf16(t[2], t[3]);
                if (a > 0) {
                    t = accQ[a] - sm_[a] * cq4;
                    qp[a][0] = pk_bf16(t[0], t[1]); qp[a][1] = pk_bf16(t[2], t[3]);
                }
            }
            f32x4 s1[5], s2[5];
            const f32x4 z = {};
            #pragma unroll
            for (int a = 1; a < 5; a++) { s1[a] = mfma16(kp[a - 1], qp[a], z); s2[a] = mfma16(kp[a], qp[a], z); }
            #pragma unroll
            for (int a = 0; a < 5; a++) {
                const f32x4 t = (accV[a] - vm[a] * cv) * vr[a];
                vp[a][0] = pk_bf16(t[0], t[1]); vp[a][1] = pk_bf16(t[2], t[3]);
            }
            sfence();
            float v0[5], v1[5], mx[5];
            #pragma unroll
            for (int a = 1; a < 5; a++) {
                const f32x4 p1 = s1[a], p2 = s2[a];
                v0[a] = sel(m0, sel(par, p1[1], p1[0]), sel(par, p2[1], p2[0]));
                v1[a] = sel(m1, sel(par, p1[3], p1[2]), sel(par, p2[3], p2[2]));
            }
            v0[1] = sel(m0 && rt == 0, NEG, v0[1]); v1[1] = sel(m1 && rt == 0, NEG, v1[1]);
            #pragma unroll
            for (int a = 1; a < 5; a++) mx[a] = fmaxf(v0[a], v1[a]);
            {
                auto a = __builtin_amdgcn_permlane16_swap(__float_as_uint(mx[1]), __float_as_uint(mx[2]), false, false);
                auto b = __builtin_amdgcn_permlane16_swap(__float_as_uint(mx[3]), __float_as_uint(mx[4]), false, false);
                const float u = fmaxf(__uint_as_float(a[0]), __uint_as_float(a[1])), v = fmaxf(__uint_as_float(b[0]), __uint_as_float(b[1]));
                auto c = __builtin_amdgcn_permlane32_swap(__float_as_uint(u), __float_as_uint(v), false, false);
                const unsigned t = __float_as_uint(fmaxf(__uint_as_float(c[0]), __uint_as_float(c[1])));
                auto d = __builtin_amdgcn_permlane32_swap(t, t, false, false);
                auto e = __builtin_amdgcn_permlane16_swap(d[0], d[0], false, false);
                auto f = __builtin_amdgcn_permlane16_swap(d[1], d[1], false, false);
                mx[1] = __uint_as_float(e[0]); mx[2] = __uint_as_float(e[1]); mx[3] = __uint_as_float(f[0]); mx[4] = __uint_as_float(f[1]);
            }
            f32x4 o[5], sum[5];
            u32x4 ones1; ones1[0] = ONES; ones1[1] = ONES; ones1[2] = ONES; ones1[3] = ONES;
            #pragma unroll
            for (int a = 1; a < 5; a++) {
                const float cs_ = CS * sr_[a];
                const unsigned pk = pk_bf16(__builtin_amdgcn_exp2f((v0[a] - mx[a]) * cs_), __builtin_amdgcn_exp2f((v1[a] - mx[a]) * cs_));
                u32x4 pf, vf;
                pf[0] = __builtin_amdgcn_perm(pk, 0u, sel0); pf[1] = __builtin_amdgcn_perm(pk, 0u, sel1);
                pf[2] = __builtin_amdgcn_perm(pk, 0u, sel2); pf[3] = __builtin_amdgcn_perm(pk, 0u, sel3);
                vf[0] = vp[a - 1][0]; vf[1] = vp[a - 1][1]; vf[2] = vp[a][0]; vf[3] = vp[a][1];
                sum[a] = mfma32(ones1, pf, z);
                o[a] = mfma32(vf, pf, z);
            }
            #pragma unroll
            for (int a = 1; a < 5; a++) {
                const f32x4 t = o[a] * __builtin_amdgcn_rcpf(sum[a][0]);
                u32x2 op; op[0] = pk_bf16(t[0], t[1]); op[1] = pk_bf16(t[2], t[3]);
                __builtin_amdgcn_raw_buffer_store_b64(op, rs_ws, o_off, (a - 1) * 16384, 16);
            }
        }

        barx();
        VMCNT(0);
        barx();

        u32x4 bo[2][16];
        #pragma unroll
        for (int k = 0; k < 16; k++) {
            LAS char* p = sm + (k >> 1) * 8192 + (k & 1) * 2048 + tr_pair;
            bo[0][k] = tr_frag(p); bo[1][k] = tr_frag(p + 8);
        }
        u32x4 b1[10][3];
        f32x4 cs[3] = {};
        u32x4 ones; ones[0] = ONES; ones[1] = ONES; ones[2] = ONES; ones[3] = ONES;
        auto absorb1 = [&](int G) {
            #pragma unroll
            for (int b = 0; b < 2; b++)
                #pragma unroll
                for (int s = 0; s < 3; s++) b1[2 * G + b][s] = tr_frag(sm + L_RING + (G % 3) * 24576 + s * 8192 + b * 2048 + tr_plain);
        };
        auto colsum1 = [&](int G) {
            #pragma unroll
            for (int b = 0; b < 2; b++)
                #pragma unroll
                for (int s = 0; s < 3; s++) cs[s] = mfma32(b1[2 * G + b][s], ones, cs[s]);
        };
        absorb1(0);
        sfence();

        bar();
        absorb1(1);
        sfence();
        u32x4 hres[2];
        unsigned h_off = WS_HT + ((rt * 4 + 2 * kh) * 16 + cb32) * 1024 + l * 16; PIN(h_off);
        {
            f32x4 acc[2][2];
            #pragma unroll
            for (int m = 0; m < 2; m++)
                #pragma unroll
                for (int ab = 0; ab < 2; ab++) {
                    acc[ab][m][0] = bf_lo(xr[m][2 * ab]); acc[ab][m][1] = bf_hi(xr[m][2 * ab]);
                    acc[ab][m][2] = bf_lo(xr[m][2 * ab + 1]); acc[ab][m][3] = bf_hi(xr[m][2 * ab + 1]);
                }
            u32x4 a2[7];
            auto RA2 = [&](int f) { a2[f % 7] = lds_r128(sm + (2 * kh + (f & 1)) * 16384 + (f >> 1) * 1024 + l * 16); };
            #pragma unroll
            for (int m = 0; m < 2; m++) { PIN(acc[0][m]); PIN(acc[1][m]); }
            sfence();
            bar();

            #pragma unroll
            for (int f = 0; f < 6; f++) { RA2(f); sfence(); }
            #pragma unroll
            for (int f = 0; f < 32; f++) {
                if (f == 10) fl_wait(1);
                MFMA_IP(acc[0][f & 1], bo[0][f >> 1], a2[f % 7]);
                if (f + 6 < 32) RA2(f + 6);
                sfence();
                MFMA_IP(acc[1][f & 1], bo[1][f >> 1], a2[f % 7]);
                if (f == 5) { fl_read(); sfence(); }
            }
            MFMA_END();

            #pragma unroll
            for (int m = 0; m < 2; m++) {
                hres[m][0] = pk_bf16(acc[0][m][0], acc[0][m][1]); hres[m][1] = pk_bf16(acc[0][m][2], acc[0][m][3]);
                hres[m][2] = pk_bf16(acc[1][m][0], acc[1][m][1]); hres[m][3] = pk_bf16(acc[1][m][2], acc[1][m][3]);
                __builtin_amdgcn_raw_buffer_store_b128(hres[m], rs_ws, h_off, m * 16384, 16);
                *(LAS u32x4*)(sm + L_XCH + j * 4096 + m * 1024 + l * 16) = hres[m];
            }
        }

        bar();
        absorb1(2); absorb1(3); absorb1(4);
        sfence();
        VMCNT(0);
        bar();

        colsum1(0); colsum1(1); colsum1(2); colsum1(3); colsum1(4);
        sfence();

        {
            f32x4 acc[3][4] = {};
            #pragma unroll
            for (int pos = 0; pos < 2; pos++)
                #pragma unroll
                for (int m = 0; m < 4; m++) {
                    const u32x4 a = lds_r128(sm + L_XCH + ((m >> 1) * 2 + pos) * 4096 + (m & 1) * 1024 + l * 16);
                    #pragma unroll
                    for (int s = 0; s < 3; s++) acc[s][m] = mfma32(b1[pos][s], a, acc[s][m]);
                }
            sfence();
            barx();
            u32x4 a3[7], br[2][3];
            float st[4];
            const int T0 = mem * 12 + j;
            unsigned f_off = WS_FT + ((rt * 4 * 43 + (T0 >> 1)) * 64 + (2 * (T0 & 1) + (g >> 1)) * 16 + i16) * 16 + (g & 1) * 8; PIN(f_off);
            auto RA = [&](int f) { a3[f % 7] = lds_r128(sm + (f & 3) * 16384 + (f >> 2) * 1024 + l * 16); };
            unsigned rb = L_RING + tr_plain; PIN(rb);
            unsigned rb2 = L_RING + 2 * 24576 + tr_plain; PIN(rb2);
            PIN(ones);
            #pragma unroll
            for (int s = 0; s < 3; s++) { PIN(cs[s]); for (int m = 0; m < 4; m++) PIN(acc[s][m]); }
            sfence();
            barx();

            #pragma unroll
            for (int f = 8; f < 14; f++) { RA(f); sfence(); }
            #pragma unroll
            for (int pos = 2; pos < 16; pos++) {
                if (pos == 6) fl_wait(2);
                if (pos == 11) fl_wait(3);
                if (pos == 13) fl_wait(4);
                const bool res = pos >= 10, nres = pos >= 9 && pos < 15;
                #pragma unroll
                for (int m = 0; m < 4; m++) {
                    const int f = pos * 4 + m;
                    const int rp = ((pos + 1) >> 1) % 3;
                    LAS char* bp = sm + (rp == 2 ? rb2 : rb) + (rp == 1 ? 24576 : 0) + ((pos + 1) & 1) * 2048;
                    u32x2 lo, hi;
                    MFMA_IP(acc[0][m], (res ? br[pos & 1][0] : b1[pos < 10 ? pos : 0][0]), a3[f % 7]);
                    if (f + 6 < 64) RA(f + 6);
                    sfence();
                    MFMA_IP(acc[1][m], (res ? br[pos & 1][1] : b1[pos < 10 ? pos : 0][1]), a3[f % 7]);
                    if (nres && m < 3) lo = tr_read(bp + m * 8192);
                    if (m == 0 && (pos == 5 || pos == 10 || pos == 12)) fl_read();
                    sfence();
                    MFMA_IP(acc[2][m], (res ? br[pos & 1][2] : b1[pos < 10 ? pos : 0][2]), a3[f % 7]);
                    if (nres && m < 3) {
                        hi = tr_read(bp + m * 8192 + 512);
                        br[(pos + 1) & 1][m][0] = lo[0]; br[(pos + 1) & 1][m][1] = lo[1]; br[(pos + 1) & 1][m][2] = hi[0]; br[(pos + 1) & 1][m][3] = hi[1];
                    }
                    sfence();
                    if (res && m < 3) MFMA_IP(cs[m], br[pos & 1][m], ones);
                }
            }
            MFMA_END();

            barx();
            #pragma unroll
            for (int m = 0; m < 4; m++) st[m] = *(LAS float*)(sm + L_S3 + (m * 16 + i16) * 8);
            #pragma unroll
            for (int m = 0; m < 4; m++)
                #pragma unroll
                for (int s = 0; s < 3; s++) {
                    const f32x4 t = acc[s][m] - st[m] * cs[s];
                    u32x2 o; o[0] = relu_pk(cvt_pk(t[0], t[1])); o[1] = relu_pk(cvt_pk(t[2], t[3]));
                    __builtin_amdgcn_raw_buffer_store_b64(o, rs_ws, f_off, T0 + s * 4 < 86 ? (unsigned)(m * (43 * 1024) + s * 2048) : OOB, 16);
                }
        }

        VMCNT(0);
        barx();

        {
            u32x4 bw[NA][2];
            float rowsc[2];
            #pragma unroll
            for (int u = 0; u < NA; u++) {
                LAS char* p = sm + (u < 8 ? u * 8192 : L_RING + (u - 8) * 8192) + kh * 2048 + tr_pair;
                bw[u][0] = tr_frag(p); bw[u][1] = tr_frag(p + 8);
            }
            #pragma unroll
            for (int m = 0; m < 2; m++) { hres[m] = lds_r128(sm + L_XCH + j * 4096 + m * 1024 + l * 16); PIN(hres[m]); rowsc[m] = *(LAS float*)(sm + L_S3 + ((2 * kh + m) * 16 + i16) * 8 + 4); PIN(rowsc[m]); }
            sfence();
            asm volatile("s_waitcnt lgkmcnt(0)" ::: "memory");
            *(volatile LAS unsigned*)(sm + L_FL + 16 + j * 4) = 1u;

            f32x4 acc[2][4] = {};
            #pragma unroll
            for (int m = 0; m < 2; m++) {
                #pragma unroll
                for (int ab = 0; ab < 2; ab++) {
                    acc[ab][m][0] = bf_lo(hres[m][2 * ab]) * rowsc[m]; acc[ab][m][1] = bf_hi(hres[m][2 * ab]) * rowsc[m];
                    acc[ab][m][2] = bf_lo(hres[m][2 * ab + 1]) * rowsc[m]; acc[ab][m][3] = bf_hi(hres[m][2 * ab + 1]) * rowsc[m];
                    PIN(acc[ab][m]);
                }
                rowsc[m] = __builtin_amdgcn_rcpf(rowsc[m]); PIN(rowsc[m]);
            }
            unsigned out_off = (((rt * 4 + 2 * kh) * 16 + i16) * 512 + cb32 * 32 + 8 * g) * 2; PIN(out_off);
            const auto rs_out = __builtin_amdgcn_make_buffer_rsrc((void*)out, 0, S_ * D_ * 2, 0x00020000);
            const unsigned a4_own = kh * 8192 + kh * 2048 + l * 16, a4_oth = (kh ^ 1) * 8192 + kh * 2048 + l * 16;
            sfence();
            barx();
            u32x4 a4[7], br[3][2];
            unsigned x_own = L_XCH + a4_own, x_oth = L_XCH + a4_oth, rb4 = L_RING + kh * 2048 + tr_pair, rb5 = rb4 + 5 * 8192;
            PIN(x_own); PIN(x_oth); PIN(rb4); PIN(rb5);
            auto R4 = [&](int f) {
                const int u = f >> 2, m = f & 3, F = u >> 1;
                a4[f % 7] = lds_r128(sm + ((F % 5) == 4 ? (m < 2 ? x_own : x_oth) : (F % 5) * 16384 + (m < 2 ? a4_own : a4_oth)) + (m & 1) * 4096 + (u & 1) * 1024);
            };
            #pragma unroll
            for (int m = 2; m < 4; m++) { PIN(acc[0][m]); PIN(acc[1][m]); }
            sfence();
            barx();

            #pragma unroll
            for (int f = 0; f < 6; f++) { R4(f); sfence(); }
            u32x2 lo;
            #pragma unroll
            for (int f = 0; f < 88; f++) {
                const int u = f >> 2, m = f & 3;
                if ((f & 7) == 2 && f < 80) barx();
                MFMA_IP(acc[0][m], (u < NA ? bw[u < NA ? u : 0][0] : br[u % 3][0]), a4[f % 7]);
                if (f + 6 < 88) R4(f + 6);
                sfence();
                MFMA_IP(acc[1][m], (u < NA ? bw[u < NA ? u : 0][1] : br[u % 3][1]), a4[f % 7]);
                if (u + 2 >= NA && u + 2 < 22) {
                    const int v = u + 2 - NA;
                    LAS char* bp = sm + (v < 5 ? rb4 : rb5) + (v < 5 ? v : v - 5) * 8192 + (m >> 1) * 8 + (m & 1) * 512;
                    if (!(m & 1)) lo = tr_read(bp);
                    else { const u32x2 hi = tr_read(bp); u32x4& d = br[(u + 2) % 3][m >> 1]; d[0] = lo[0]; d[1] = lo[1]; d[2] = hi[0]; d[3] = hi[1]; }
                    sfence();
                }
            }
            MFMA_END();

            #pragma unroll
            for (int ab = 0; ab < 2; ab++)
                #pragma unroll
                for (int m = 0; m < 2; m++) *(LAS f32x4*)(sm + L_XCH + j * 4096 + (ab * 2 + m) * 1024 + l * 16) = acc[ab][2 + m];
            bar();
            #pragma unroll
            for (int m = 0; m < 2; m++) {
                const f32x4 ta = (acc[0][m] + *(LAS f32x4*)(sm + L_XCH + (j ^ 2) * 4096 + m * 1024 + l * 16)) * rowsc[m];
                const f32x4 tb = (acc[1][m] + *(LAS f32x4*)(sm + L_XCH + (j ^ 2) * 4096 + (2 + m) * 1024 + l * 16)) * rowsc[m];
                u32x4 o;
                o[0] = pk_bf16(ta[0], ta[1]); o[1] = pk_bf16(ta[2], ta[3]); o[2] = pk_bf16(tb[0], tb[1]); o[3] = pk_bf16(tb[2], tb[3]);
                __builtin_amdgcn_raw_buffer_store_b128(o, rs_out, out_off, m * 16384, 0);
            }
        }

    }
}

extern "C" void kernel_launch(void* const* d_in, const int* in_sizes, int n_in, void* d_out, int out_size, void* d_ws, size_t ws_size, hipStream_t stream) {
    (void)in_sizes; (void)n_in; (void)out_size; (void)ws_size;
    (void)hipFuncSetAttribute((const void*)fused_block_kernel, hipFuncAttributeMaxDynamicSharedMemorySize, LDS_BYTES);
    fused_block_kernel<<<256, 512, LDS_BYTES, stream>>>((const u16*)d_in[0], (const u16*)d_in[1], (const u16*)d_in[2], (const u16*)d_in[4], (const u16*)d_in[5], (u16*)d_out, (char*)d_ws);
}
```
